```python
import torch

def _softmax(x, axis=-1):
    return torch.softmax(x, dim=axis)
def _gelu(x):
    return torch.nn.functional.gelu(x, approximate="tanh")
def _silu(x):
    return torch.nn.functional.silu(x)
def _sigmoid(x):
    return torch.sigmoid(x)
def _layernorm(x, axis=-1, eps=1e-5):
    m = x.mean(dim=axis, keepdim=True)
    v = x.var(dim=axis, keepdim=True, unbiased=False)
    return (x - m) / torch.sqrt(v + eps)
def _scatter_add(tgt, idx, src, axis=0):
    return tgt.clone().index_add_(axis, idx, src)
def _segment_sum(src, seg, n):
    out = torch.zeros((n,) + src.shape[1:], dtype=src.dtype)
    return out.index_add_(0, seg, src)
def _pad(x, pw):
    flat = tuple(v for pair in reversed(pw) for v in pair)
    return torch.nn.functional.pad(x, flat)

kernel_name = 'parallel_sum__compressed_kv_cla__glu_gated_swiglu__decode'

def setup_inputs():

    _w = (lambda a: (lambda t: t.to(torch.bfloat16) if t.is_floating_point() else t)(torch.as_tensor(a)))
    def _A_setup():
        B, H, Sq, Sk, D, M = 2, 32, 1, 8192, 128, 512
        Dc = 4 * D
        return {
            "x": _w((lambda *s: torch.randn(*s, dtype=torch.bfloat16) * 0.05)(B, Sq, M)),
            "c_kv": _w((lambda *s: torch.randn(*s, dtype=torch.bfloat16) * 0.8)(B, Sk, Dc)),
            "w_q": _w((lambda *s: torch.randn(*s, dtype=torch.bfloat16) * 1.6)(M, H, D) * (M ** -0.5)),
            "w_uk": _w((lambda *s: torch.randn(*s, dtype=torch.bfloat16) * 1.6)(Dc, H, D) * (Dc ** -0.5)),
            "w_uv": _w((lambda *s: torch.randn(*s, dtype=torch.bfloat16) * 0.8)(Dc, H, D) * (Dc ** -0.5)),
            "w_o": _w((lambda *s: torch.randn(*s, dtype=torch.bfloat16) * 0.8)(H, D, M) * ((H * D) ** -0.5)),
        }
    _A_in = _A_setup()

    def _B_setup():
        B, S, D, F = 2, 1, 512, 1376
        return {
            "x": _w((lambda *s: torch.randn(*s, dtype=torch.bfloat16) * 0.05)(B, S, D)),
            "w1": _w((lambda *s: torch.randn(*s, dtype=torch.bfloat16) * 0.2)(D, F) * (2.0 / D) ** 0.5),
            "w3": _w((lambda *s: torch.randn(*s, dtype=torch.bfloat16) * 0.2)(D, F) * (2.0 / D) ** 0.5),
            "w2": _w((lambda *s: torch.randn(*s, dtype=torch.bfloat16) * 0.2)(F, D) * (2.0 / F) ** 0.5),
        }
    _B_in = _B_setup()

    return {
        "A_x": _A_in["x"],
        "A_c_kv": _A_in["c_kv"],
        "A_w_q": _A_in["w_q"],
        "A_w_uk": _A_in["w_uk"],
        "A_w_uv": _A_in["w_uv"],
        "A_w_o": _A_in["w_o"],
        "B_x": _B_in["x"],
        "B_w1": _B_in["w1"],
        "B_w3": _B_in["w3"],
        "B_w2": _B_in["w2"],
    }

def reference(A_x, A_c_kv, A_w_q, A_w_uk, A_w_uv, A_w_o, B_x, B_w1, B_w3, B_w2):
    def _A_ref(x, c_kv, w_q, w_uk, w_uv, w_o):
        B, Sq, M = x.shape[0], x.shape[1], x.shape[2]
        Sk = c_kv.shape[1]
        H, D = w_q.shape[1], w_q.shape[2]
        q = torch.einsum("bqm,mhd->bhqd", x, w_q)
        k = torch.einsum("bkc,chd->bhkd", c_kv, w_uk)
        v = torch.einsum("bkc,chd->bhkd", c_kv, w_uv)
        scores = torch.einsum("bhqd,bhkd->bhqk", q, k) * (D ** -0.5)
        off = Sk - Sq
        qi = torch.arange(Sq)[:, None]
        kj = torch.arange(Sk)[None, :]
        cmask = kj <= (qi + off)
        scores = torch.where(cmask[None, None], scores, -1e9)
        p = _softmax(scores, axis=-1)
        ctx = torch.einsum("bhqk,bhkd->bhqd", p, v)
        return torch.einsum("bhqd,hdm->bqm", ctx, w_o)

    def _B_ref(x, w1, w3, w2):
        gate = torch.einsum("bsd,df->bsf", x, w1)
        up = torch.einsum("bsd,df->bsf", x, w3)
        h = _silu(gate) * up
        out = torch.einsum("bsf,fd->bsd", h, w2)
        return out

    x = A_x
    xn = _layernorm(x, axis=-1)
    return x + _A_ref(xn, A_c_kv, A_w_q, A_w_uk, A_w_uv, A_w_o) + _B_ref(xn, B_w1, B_w3, B_w2)
```

```cpp
#include <hip/hip_runtime.h>
#include <hip/hip_bf16.h>
#include <stdint.h>

constexpr int B=2,H=32,SK=8192,D=128,M=512,DC=512,F=1376;
typedef unsigned short u16;
__device__ __forceinline__ float bf2f(u16 v){ return __uint_as_float(((unsigned)v)<<16); }
__device__ __forceinline__ u16 f2bf(float f){ unsigned u=__float_as_uint(f); u+=0x7FFFu+((u>>16)&1u); return (u16)(u>>16);}
__device__ __forceinline__ float rbf(float f){ return bf2f(f2bf(f)); }

__device__ void layernorm512(const u16* x, float* xn  , float* red  ){
  int t=threadIdx.x;
  float a=bf2f(x[t]), b=bf2f(x[t+256]);
  float s=a+b;
  for(int o=32;o;o>>=1) s+=__shfl_xor(s,o);
  if((t&63)==0) red[t>>6]=s;
  __syncthreads();
  float m=(red[0]+red[1]+red[2]+red[3])*(1.f/512.f);
  __syncthreads();
  float da=a-m, db=b-m;
  float v=da*da+db*db;
  for(int o=32;o;o>>=1) v+=__shfl_xor(v,o);
  if((t&63)==0) red[t>>6]=v;
  __syncthreads();
  float var=(red[0]+red[1]+red[2]+red[3])*(1.f/512.f);
  float mb=rbf(m); float vb=rbf(var);
  float den=rbf(sqrtf(rbf(vb+1e-5f)));
  xn[t]=rbf(rbf(a-mb)/den); xn[t+256]=rbf(rbf(b-mb)/den);
  __syncthreads();
}

__global__ void k1(const u16* x,const u16* wq,const u16* wuk,float* qabs){
  __shared__ float xn[512]; __shared__ float red[4]; __shared__ float q[128];
  int b=blockIdx.x/H, h=blockIdx.x%H; int t=threadIdx.x;
  layernorm512(x+b*M,xn,red);
  if(t<128){ float s=0; for(int m=0;m<M;m++) s+=xn[m]*bf2f(wq[(size_t)m*H*D+h*D+t]); q[t]=rbf(s);}
  __syncthreads();
  for(int c=t;c<DC;c+=256){ float s=0; for(int d=0;d<D;d++) s+=q[d]*bf2f(wuk[(size_t)c*H*D+h*D+d]); qabs[(b*H+h)*DC+c]=s; }
}
constexpr int NS=128;
__global__ void k2(const u16* ckv,const float* qabs,float* po,float* pm,float* pl){
  __shared__ float sc[32][64]; __shared__ u16 ck[64][512+8];
  int b=blockIdx.x/NS, s=blockIdx.x%NS; int t=threadIdx.x;
  const u16* base=ckv+((size_t)b*SK+s*64)*DC;
  for(int i=t;i<64*512;i+=256) ck[i/512][i%512]=base[i];
  __syncthreads();
  for(int i=t;i<32*64;i+=256){ int h=i/64,k=i%64; float a=0; const float* qa=qabs+(b*H+h)*DC; for(int c=0;c<DC;c++) a+=qa[c]*bf2f(ck[k][c]); sc[h][k]=a*0.08838834764831845f; }
  __syncthreads();
  if(t<32){ float m=-1e30f; for(int k=0;k<64;k++) m=fmaxf(m,sc[t][k]); float l=0; for(int k=0;k<64;k++){ float p=__expf(sc[t][k]-m); sc[t][k]=p; l+=p;} pm[(b*H+t)*NS+s]=m; pl[(b*H+t)*NS+s]=l; }
  __syncthreads();
  for(int i=t;i<32*512;i+=256){ int h=i/512,c=i%512; float a=0; for(int k=0;k<64;k++) a+=sc[h][k]*bf2f(ck[k][c]); po[((size_t)(b*H+h)*NS+s)*DC+c]=a; }
}
__global__ void k3(const float* po,const float* pm,const float* pl,const u16* wuv,float* ctx){
  __shared__ float w[NS]; __shared__ float cl[512];
  int b=blockIdx.x/H,h=blockIdx.x%H; int t=threadIdx.x;
  float M_=-1e30f; for(int s=0;s<NS;s++) M_=fmaxf(M_,pm[(b*H+h)*NS+s]);
  float L=0; for(int s=0;s<NS;s++) L+=pl[(b*H+h)*NS+s]*__expf(pm[(b*H+h)*NS+s]-M_);
  if(t<NS) w[t]=__expf(pm[(b*H+h)*NS+t]-M_)/L;
  __syncthreads();
  for(int c=t;c<DC;c+=256){ float a=0; for(int s=0;s<NS;s++) a+=w[s]*po[((size_t)(b*H+h)*NS+s)*DC+c]; cl[c]=a; }
  __syncthreads();
  if(t<128){ float a=0; for(int c=0;c<DC;c++) a+=cl[c]*bf2f(wuv[(size_t)c*H*D+h*D+t]); ctx[(b*H+h)*D+t]=rbf(a);}
}
__global__ void k4(const u16* x,const u16* w1,const u16* w3,float* hh){
  __shared__ float xn[512]; __shared__ float red[4];
  int b=blockIdx.y; int t=threadIdx.x; int f=blockIdx.x*256+t;
  layernorm512(x+b*M,xn,red);
  if(f<F){ float g=0,u=0; for(int m=0;m<M;m++){ g+=xn[m]*bf2f(w1[(size_t)m*F+f]); u+=xn[m]*bf2f(w3[(size_t)m*F+f]); } g=rbf(g); u=rbf(u); float si=rbf(g/(1.f+__expf(-g))); hh[b*F+f]=rbf(si*u);}
}
__global__ void k5(const u16* x,const float* ctx,const float* hh,const u16* wo,const u16* w2,u16* out){
  int b=blockIdx.y; int n=blockIdx.x*64+threadIdx.x;
  float a=0; for(int k=0;k<H*D;k++) a+=ctx[b*H*D+k]*bf2f(wo[(size_t)k*M+n]);
  float f=0; for(int k=0;k<F;k++) f+=hh[b*F+k]*bf2f(w2[(size_t)k*M+n]);
  out[b*M+n]=f2bf(rbf(bf2f(x[b*M+n])+rbf(a))+rbf(f));
}
extern "C" void kernel_launch(void* const* d_in, const int* in_sizes, int n_in, void* d_out, int out_size, void* d_ws, size_t ws_size, hipStream_t stream){
  const u16* x=(const u16*)d_in[0]; const u16* ckv=(const u16*)d_in[1]; const u16* wq=(const u16*)d_in[2]; const u16* wuk=(const u16*)d_in[3]; const u16* wuv=(const u16*)d_in[4]; const u16* wo=(const u16*)d_in[5];
  const u16* w1=(const u16*)d_in[7]; const u16* w3=(const u16*)d_in[8]; const u16* w2=(const u16*)d_in[9];
  float* ws=(float*)d_ws;
  float* qabs=ws; float* po=qabs+B*H*DC; float* pm=po+(size_t)B*H*NS*DC; float* pl=pm+B*H*NS; float* ctx=pl+B*H*NS; float* hh=ctx+B*H*D;
  k1<<<B*H,256,0,stream>>>(x,wq,wuk,qabs);
  k2<<<B*NS,256,0,stream>>>(ckv,qabs,po,pm,pl);
  k3<<<B*H,256,0,stream>>>(po,pm,pl,wuv,ctx);
  k4<<<dim3((F+255)/256,B),256,0,stream>>>(x,w1,w3,hh);
  k5<<<dim3(M/64,B),64,0,stream>>>(x,ctx,hh,wo,w2,(u16*)d_out);
}
```

```cpp
#include <hip/hip_runtime.h>
#include <stdint.h>

typedef unsigned short u16;
typedef unsigned int u32;
typedef unsigned long long u64;
typedef __attribute__((ext_vector_type(4))) float f32x4;
typedef __attribute__((ext_vector_type(8))) short s16x8;
typedef __attribute__((ext_vector_type(4))) short s16x4;
typedef __attribute__((ext_vector_type(4))) u32 u32x4;
typedef __attribute__((ext_vector_type(2))) u32 u32x2;
typedef __attribute__((ext_vector_type(2))) __bf16 bf16x2;
#define GAS __attribute__((address_space(1)))
#define LAS __attribute__((address_space(3)))
#define CAS __attribute__((address_space(4)))

constexpr int SK = 8192, FF = 1376;
constexpr u32 WS_QABS = 4096;
constexpr u32 WS_INA = 1u << 20;
constexpr u32 INA_STRIDE = 16896;
constexpr u32 WS_INB = 8u << 20;
constexpr u32 WS_STAMP = 16u << 20;
constexpr u32 WS_BYTES = 32u << 20;
constexpr u32 L_C = 0;
constexpr u32 L_QA = 131072;
constexpr u32 L_P = 147456;
constexpr u32 L_XN = 151552;
constexpr u32 L_QB = 153600;
constexpr u32 L_MAX = 155648;
constexpr u32 L_FLAG = 156160;
constexpr u32 L_TOTAL = 156224;
constexpr u32 L_R1 = 0;
constexpr u32 L_R2 = 4096;
constexpr u32 L_R3 = 12288;
constexpr u32 L_FP = 16384;
constexpr u32 L_R4 = 17408;

constexpr int SPIN_MAX = 1 << 18;
constexpr u32 OOB = 0x7ffffff0u;

__device__ __forceinline__ float bf_lo(u32 v) { return __uint_as_float(v << 16); }
__device__ __forceinline__ float bf_hi(u32 v) { return __uint_as_float(v & 0xffff0000u); }
__device__ __forceinline__ u32 pk_bf16(float a, float b) {
  bf16x2 v = {(__bf16)a, (__bf16)b};
  return __builtin_bit_cast(u32, v);
}
__device__ __forceinline__ float rbf(float a) { return bf_lo(pk_bf16(a, a)); }
template <int CTRL> __device__ __forceinline__ float dpp(float v) {
  return __int_as_float(__builtin_amdgcn_update_dpp(0, __float_as_int(v), CTRL, 0xf, 0xf, true));
}
__device__ __forceinline__ float swap16_add(float v) {
  auto r = __builtin_amdgcn_permlane16_swap(__float_as_uint(v), __float_as_uint(v), false, false);
  return __uint_as_float(r[0]) + __uint_as_float(r[1]);
}
__device__ __forceinline__ float swap32_add(float v) {
  auto r = __builtin_amdgcn_permlane32_swap(__float_as_uint(v), __float_as_uint(v), false, false);
  return __uint_as_float(r[0]) + __uint_as_float(r[1]);
}
__device__ __forceinline__ float swap16_max(float v) {
  auto r = __builtin_amdgcn_permlane16_swap(__float_as_uint(v), __float_as_uint(v), false, false);
  return fmaxf(__uint_as_float(r[0]), __uint_as_float(r[1]));
}
__device__ __forceinline__ float swap32_max(float v) {
  auto r = __builtin_amdgcn_permlane32_swap(__float_as_uint(v), __float_as_uint(v), false, false);
  return fmaxf(__uint_as_float(r[0]), __uint_as_float(r[1]));
}
__device__ __forceinline__ float wave_sum(float v) {
  v += dpp<0x128>(v); v += dpp<0x124>(v); v += dpp<0x122>(v); v += dpp<0x121>(v);
  return swap32_add(swap16_add(v));
}
__device__ __forceinline__ float wave_max(float v) {
  v = fmaxf(v, dpp<0x128>(v)); v = fmaxf(v, dpp<0x124>(v)); v = fmaxf(v, dpp<0x122>(v)); v = fmaxf(v, dpp<0x121>(v));
  return swap32_max(swap16_max(v));
}
__device__ __forceinline__ f32x4 mfma(u32x4 a, u32x4 b, f32x4 c) {
  return __builtin_amdgcn_mfma_f32_16x16x32_bf16(__builtin_bit_cast(s16x8, a), __builtin_bit_cast(s16x8, b), c, 0, 0, 0);
}
__device__ __forceinline__ void lds_barrier() { asm volatile("s_waitcnt lgkmcnt(0)\n\ts_barrier" ::: "memory"); }
__device__ __forceinline__ void cfence() { asm volatile("" ::: "memory"); __builtin_amdgcn_sched_barrier(0); }

#define LP(T, off) ((LAS T*)(lds + (off)))
#define LDW(p) (*(const GAS u32x4*)(p))

__device__ __forceinline__ void flag_set(u32 addr, u32 tag) {
  asm volatile("ds_write_b32 %0, %1" ::"v"(addr), "v"(tag) : "memory");
}
__device__ __forceinline__ void flag_wait4(u32 addr, u32 tag) {
  for (int it = 0; it < SPIN_MAX; ++it) {
    u32x4 v;
    asm volatile("ds_read_b128 %0, %1\n\ts_waitcnt lgkmcnt(0)" : "=v"(v) : "v"(addr) : "memory");
    if (v[0] == tag && v[1] == tag && v[2] == tag && v[3] == tag) break;
    __builtin_amdgcn_s_sleep(1);
  }
}
__device__ __forceinline__ u32x4 maskfrag(u32 d, const u32x4& sel) {
  u32x4 f;
  f[0] = __builtin_amdgcn_perm(d, d, sel[0]); f[1] = __builtin_amdgcn_perm(d, d, sel[1]);
  f[2] = __builtin_amdgcn_perm(d, d, sel[2]); f[3] = __builtin_amdgcn_perm(d, d, sel[3]);
  return f;
}

#define STAMP(k) do {} while (0)
#define STAMP_DECL do {} while (0)
#define STAMP_FLUSH() do {} while (0)

__global__ __launch_bounds__(512, 2) void mla_ffn_kernel(
    const u16* __restrict__ x, const u16* __restrict__ ckv, const u16* __restrict__ wq, const u16* __restrict__ wuk,
    const u16* __restrict__ wuv, const u16* __restrict__ wo, const u16* __restrict__ w1, const u16* __restrict__ w3,
    const u16* __restrict__ w2, u16* __restrict__ out, unsigned char* __restrict__ ws) {
  __shared__ __attribute__((aligned(16))) unsigned char lds_raw[L_TOTAL];
  LAS unsigned char* lds = (LAS unsigned char*)lds_raw;
  const u32 ldsb = (u32)(uintptr_t)lds;
  const int tid = threadIdx.x, lane = tid & 63;
  const int w = __builtin_amdgcn_readfirstlane(tid >> 6);
  const int bid = blockIdx.x, X = bid & 7, I = bid >> 3;
  const int h = 4 * X + (I >> 3), j = I & 7;
  const int pair = 16 * X + (I >> 1), b = pair >> 6, s = pair & 63, G = I & 1;
  const bool is_final = (I < 16) && ((I & 1) != (X >> 2));
  const int fin = X * 8 + (I >> 1);
  const int fu = 22 * X + (I - 10);
  const bool has_ffn = (I >= 10) && (fu < 172);
  const int g = lane >> 4, r = lane & 15, jj = lane & 7, bsel = (lane >> 3) & 1;
  STAMP_DECL;
  STAMP(0);
  const u32 tag = *(const CAS u32*)ws + 1u;
  asm volatile("" ::"s"(wuv), "s"(wo), "s"(w1), "s"(w3), "s"(w2), "s"(out), "s"(x), "s"(ckv), "s"(wq), "s"(wuk));
  const __amdgpu_buffer_rsrc_t w1r = __builtin_amdgcn_make_buffer_rsrc((void*)w1, 0, 512 * FF * 2, 0x00020000);
  const __amdgpu_buffer_rsrc_t w3r = __builtin_amdgcn_make_buffer_rsrc((void*)w3, 0, 512 * FF * 2, 0x00020000);
  const __amdgpu_buffer_rsrc_t w2r = __builtin_amdgcn_make_buffer_rsrc((void*)w2, 0, 512 * FF * 2, 0x00020000);
  const __amdgpu_buffer_rsrc_t wsr = __builtin_amdgcn_make_buffer_rsrc((void*)ws, 0, WS_BYTES, 0x00020000);

  u32x4 sel_lo, sel_hi;
#pragma unroll
  for (int k = 0; k < 4; ++k) {
    const bool on = (k == (jj >> 1));
    sel_lo[k] = on ? ((jj & 1) ? 0x01000c0cu : 0x0c0c0100u) : 0x0c0c0c0cu;
    sel_hi[k] = on ? ((jj & 1) ? 0x03020c0cu : 0x0c0c0302u) : 0x0c0c0c0cu;
  }

  if (w < 4) {
    const u32x4 x0 = LDW(x + 8 * lane), x1 = LDW(x + 512 + 8 * lane);
    u32x4 wqv[32], wukv[4];
    {
      const u16* p = wq + (size_t)(128 * w + 32 * g) * 4096 + h * 128 + r * 8;
#pragma unroll
      for (int t = 0; t < 32; ++t) wqv[t] = LDW(p + (size_t)t * 4096);
      const u16* pk = wuk + (size_t)(64 * j + 16 * w + r) * 4096 + h * 128 + g * 8;
#pragma unroll
      for (int k = 0; k < 4; ++k) wukv[k] = LDW(pk + k * 32);
    }
    cfence();
    STAMP(1);
    {
      float a[8], c[8];
#pragma unroll
      for (int k = 0; k < 4; ++k) { a[2 * k] = bf_lo(x0[k]); a[2 * k + 1] = bf_hi(x0[k]); c[2 * k] = bf_lo(x1[k]); c[2 * k + 1] = bf_hi(x1[k]); }
      float sa = 0.f, sc = 0.f;
#pragma unroll
      for (int k = 0; k < 8; ++k) { sa += a[k]; sc += c[k]; }
      sa = wave_sum(sa) * (1.f / 512.f); sc = wave_sum(sc) * (1.f / 512.f);
      float va = 0.f, vc = 0.f;
#pragma unroll
      for (int k = 0; k < 8; ++k) { const float da = a[k] - sa, dc = c[k] - sc; va += da * da; vc += dc * dc; }
      va = wave_sum(va) * (1.f / 512.f); vc = wave_sum(vc) * (1.f / 512.f);
      const float ma = rbf(sa), mc = rbf(sc);
      const float dena = rbf(sqrtf(rbf(rbf(va) + 1e-5f))), denc = rbf(sqrtf(rbf(rbf(vc) + 1e-5f)));
      u32x4 n0, n1;
#pragma unroll
      for (int k = 0; k < 4; ++k) {
        n0[k] = pk_bf16(rbf(a[2 * k] - ma) / dena, rbf(a[2 * k + 1] - ma) / dena);
        n1[k] = pk_bf16(rbf(c[2 * k] - mc) / denc, rbf(c[2 * k + 1] - mc) / denc);
      }
      *LP(u32x4, L_XN + 16 * lane) = n0;
      *LP(u32x4, L_XN + 1024 + 16 * lane) = n1;
    }
    STAMP(2);
    u32x4 xf[4];
#pragma unroll
    for (int u = 0; u < 4; ++u) xf[u] = *LP(u32x4, L_XN + bsel * 1024 + (128 * w + 32 * g) * 2 + 16 * u);
    f32x4 q0 = {0.f, 0.f, 0.f, 0.f}, q1 = {0.f, 0.f, 0.f, 0.f};
#pragma unroll
    for (int t = 0; t < 32; t += 2) {
      const u32 d = xf[t >> 3][(t >> 1) & 3];
      q0 = mfma(wqv[t], maskfrag(d, sel_lo), q0);
      q1 = mfma(wqv[t + 1], maskfrag(d, sel_hi), q1);
    }
    q0 += q1;
    *LP(f32x4, L_P + w * 1024 + 16 * lane) = q0;
    flag_set(ldsb + L_FLAG + 4 * w, tag);
    STAMP(3);
    flag_wait4(ldsb + L_FLAG, tag);
    {
      f32x4 q = *LP(f32x4, L_P + 16 * lane);
      q += *LP(f32x4, L_P + 1024 + 16 * lane);
      q += *LP(f32x4, L_P + 2048 + 16 * lane);
      q += *LP(f32x4, L_P + 3072 + 16 * lane);
      const u32 p01 = pk_bf16(q[0], q[1]), p23 = pk_bf16(q[2], q[3]);
      LAS u16* qb = LP(u16, L_QB + w * 512 + bsel * 256 + (32 * g + jj) * 2);
      qb[0] = (u16)p01; qb[8] = (u16)(p01 >> 16); qb[16] = (u16)p23; qb[24] = (u16)(p23 >> 16);
    }
    f32x4 qa = {0.f, 0.f, 0.f, 0.f};
#pragma unroll
    for (int k = 0; k < 4; ++k) {
      const u32x4 bq = *LP(u32x4, L_QB + w * 512 + (r & 1) * 256 + (32 * k + 8 * g) * 2);
      qa = mfma(wukv[k], bq, qa);
    }
    const float qs = 0.08838834764831845f * 1.4426950408889634f;
    u32x4 gr;
    gr[0] = pk_bf16(qa[0] * qs, qa[1] * qs); gr[1] = tag; gr[2] = pk_bf16(qa[2] * qs, qa[3] * qs); gr[3] = tag;
    flag_wait4(ldsb + L_FLAG + 16, tag);
    if (r < 2)
      __builtin_amdgcn_raw_buffer_store_b128(gr, wsr, WS_QABS + ((r * 32 + h) * 256 + 32 * j + 8 * w + 2 * g) * 8, 0, 16);
    STAMP(4);
  } else {
    const int t = tid - 256;
    const u32x4 z = {0u, 0u, 0u, 0u};
    const u32 ia = WS_INA + (h * 8 + j) * INA_STRIDE + t * 16;
#pragma unroll
    for (int k = 0; k < 4; ++k) __builtin_amdgcn_raw_buffer_store_b128(z, wsr, ia + k * 4096, 0, 16);
    if (t < 32) __builtin_amdgcn_raw_buffer_store_b128(z, wsr, ia + 16384, 0, 16);
    if (is_final) {
      const u32 ib = WS_INB + fin * 16384 + t * 16;
#pragma unroll
      for (int k = 0; k < 4; ++k) __builtin_amdgcn_raw_buffer_store_b128(z, wsr, ib + k * 4096, 0, 16);
    }
    cfence();
    const int u = w - 4;
    const u16* cbase = ckv + ((size_t)b * SK + s * 128 + 32 * u) * 512;
#pragma unroll
    for (int i = 0; i < 32; ++i) {
      const u32 voff = i * 1024 + 16 * (lane ^ (2 * (i & 7)));
      const u32 dst = ldsb + L_C + (32 * u + i) * 1024;
      asm volatile("s_mov_b32 m0, %2\n\ts_nop 0\n\tglobal_load_lds_dwordx4 %0, %1" ::"v"(voff), "s"(cbase), "s"(dst) : "memory");
    }
    STAMP(1);
    asm volatile("s_waitcnt vmcnt(32)" ::: "memory");
    flag_set(ldsb + L_FLAG + 16 + 4 * u, tag);
    STAMP(2);
    flag_wait4(ldsb + L_FLAG + 32, tag);
    STAMP(3);
  }

  u32x4 gq[4];
  const u32 gqo = WS_QABS + ((b * 32 + 16 * G + 2 * w) * 256 + 2 * lane) * 8;
#pragma unroll
  for (int u = 0; u < 4; ++u) gq[u] = __builtin_amdgcn_raw_buffer_load_b128(wsr, gqo + (u >> 1) * 2048 + (u & 1) * 1024, 0, 16);
  cfence();
  if (w < 4) flag_set(ldsb + L_FLAG + 32 + 4 * w, tag);
  u32x4 wuvv[2], wov[16], w1v, w3v, w2v;
  const int nq = w & 3, rh = w >> 2;
#define PREFETCH() do { \
    const u16* pv = wuv + (size_t)(64 * j + 8 * w + g) * 4096 + h * 128 + r * 8; \
    wuvv[0] = LDW(pv); wuvv[1] = LDW(pv + 4 * 4096); \
    const u16* po = wo + (size_t)(h * 128 + 64 * rh + 16 * g) * 512 + 128 * nq + 8 * r; \
    _Pragma("unroll") for (int t = 0; t < 16; ++t) wov[t] = LDW(po + t * 512); \
    w1v = __builtin_amdgcn_raw_buffer_load_b128(w1r, has_ffn ? (u32)(tid * FF + 8 * fu) * 2u : OOB, 0, 0); \
    w3v = __builtin_amdgcn_raw_buffer_load_b128(w3r, has_ffn ? (u32)(tid * FF + 8 * fu) * 2u : OOB, 0, 0); \
    w2v = __builtin_amdgcn_raw_buffer_load_b128(w2r, has_ffn ? (u32)((8 * fu + 4 * rh + g) * 512 + 128 * nq + 8 * r) * 2u : OOB, 0, 0); \
  } while (0)
  PREFETCH();
  cfence();
  for (int it = 0; it < SPIN_MAX; ++it) {
    bool k[4];
#pragma unroll
    for (int u = 0; u < 4; ++u) k[u] = (gq[u][1] == tag) & (gq[u][3] == tag);
    if (__all(k[0] & k[1] & k[2] & k[3])) break;
    __builtin_amdgcn_s_sleep(1);
    u32x4 nw[4];
#pragma unroll
    for (int u = 0; u < 4; ++u) nw[u] = __builtin_amdgcn_raw_buffer_load_b128(wsr, k[u] ? OOB : gqo + (u >> 1) * 2048 + (u & 1) * 1024, 0, 16);
    cfence();
#pragma unroll
    for (int u = 0; u < 4; ++u) gq[u] = k[u] ? gq[u] : nw[u];
  }
  STAMP(5);
#pragma unroll
  for (int u = 0; u < 4; ++u) {
    const int hl = 2 * w + (u >> 1), idx = (u & 1) * 64 + lane;
    *LP(u32x2, L_QA + hl * 1024 + 16 * ((idx >> 1) ^ (2 * (hl & 7))) + 8 * (idx & 1)) = u32x2{gq[u][0], gq[u][2]};
  }
  lds_barrier();
  STAMP(6);

  float pm, pl;
  f32x4 sc;
  {
    f32x4 s0 = {0.f, 0.f, 0.f, 0.f}, s1 = {0.f, 0.f, 0.f, 0.f};
    const int z = 2 * (r & 7);
#pragma unroll
    for (int kk = 0; kk < 16; kk += 2) {
      const u32 o0 = 16 * ((4 * kk + g) ^ z), o1 = 16 * ((4 * kk + 4 + g) ^ z);
      s0 = mfma(*LP(u32x4, L_C + (16 * w + r) * 1024 + o0), *LP(u32x4, L_QA + r * 1024 + o0), s0);
      s1 = mfma(*LP(u32x4, L_C + (16 * w + r) * 1024 + o1), *LP(u32x4, L_QA + r * 1024 + o1), s1);
    }
    sc = s0 + s1;
    float m = fmaxf(fmaxf(sc[0], sc[1]), fmaxf(sc[2], sc[3]));
    m = swap32_max(swap16_max(m));
    if (g == 0) *LP(float, L_MAX + (r * 8 + w) * 4) = m;
  }
  lds_barrier();
  {
    const f32x4 m0 = *LP(f32x4, L_MAX + r * 32), m1 = *LP(f32x4, L_MAX + r * 32 + 16);
    pm = fmaxf(fmaxf(fmaxf(m0[0], m0[1]), fmaxf(m0[2], m0[3])), fmaxf(fmaxf(m1[0], m1[1]), fmaxf(m1[2], m1[3])));
    const float p0 = __builtin_amdgcn_exp2f(sc[0] - pm), p1 = __builtin_amdgcn_exp2f(sc[1] - pm);
    const float p2 = __builtin_amdgcn_exp2f(sc[2] - pm), p3 = __builtin_amdgcn_exp2f(sc[3] - pm);
    *LP(u32x2, L_P + (w >> 1) * 1024 + lane * 16 + (w & 1) * 8) = u32x2{pk_bf16(p0, p1), pk_bf16(p2, p3)};
  }
  lds_barrier();
  STAMP(7);
  {
    u32x4 pf[4];
#pragma unroll
    for (int ks = 0; ks < 4; ++ks) pf[ks] = *LP(u32x4, L_P + ks * 1024 + lane * 16);
    f32x4 acc[4], accl = {0.f, 0.f, 0.f, 0.f};
    const u32x4 ones = {0x3f803f80u, 0x3f803f80u, 0x3f803f80u, 0x3f803f80u};
    const int rr = r >> 2, cc = r & 3, z = 2 * (4 * (g & 1) + rr);
#pragma unroll
    for (int ct = 0; ct < 4; ++ct) {
      acc[ct] = f32x4{0.f, 0.f, 0.f, 0.f};
      const u32 o = L_C + (4 * g + rr) * 1024 + 16 * ((8 * w + 2 * ct + (cc >> 1)) ^ z) + 8 * (cc & 1);
#pragma unroll
      for (int ks = 0; ks < 4; ++ks) {
        const s16x4 lo = __builtin_amdgcn_ds_read_tr16_b64_v4i16(LP(s16x4, o + ks * 32768));
        const s16x4 hi = __builtin_amdgcn_ds_read_tr16_b64_v4i16(LP(s16x4, o + ks * 32768 + 16384));
        const u32x2 l2 = __builtin_bit_cast(u32x2, lo), h2 = __builtin_bit_cast(u32x2, hi);
        acc[ct] = mfma(u32x4{l2[0], l2[1], h2[0], h2[1]}, pf[ks], acc[ct]);
      }
    }
#pragma unroll
    for (int ks = 0; ks < 4; ++ks) accl = mfma(ones, pf[ks], accl);
    pl = accl[0];
    const float inv = 1.f / pl;
    const u32 dst = WS_INA + ((16 * G + r) * 8 + w) * INA_STRIDE;
#pragma unroll
    for (int hf = 0; hf < 2; ++hf) {
      u32x4 o;
      o[0] = pk_bf16(acc[2 * hf][0] * inv, acc[2 * hf][1] * inv) | 1u;
      o[1] = pk_bf16(acc[2 * hf][2] * inv, acc[2 * hf][3] * inv) | 1u;
      o[2] = pk_bf16(acc[2 * hf + 1][0] * inv, acc[2 * hf + 1][1] * inv) | 1u;
      o[3] = pk_bf16(acc[2 * hf + 1][2] * inv, acc[2 * hf + 1][3] * inv) | 1u;
      __builtin_amdgcn_raw_buffer_store_b128(o, wsr, dst + (b * 64 + s) * 128 + hf * 64 + g * 16, 0, 16);
    }
    if (g == 0) {
      const float lse = pm + __builtin_amdgcn_logf(pl);
      __builtin_amdgcn_raw_buffer_store_b32(__float_as_uint(lse) | 1u, wsr, dst + 16384 + (s * 2 + b) * 4, 0, 16);
    }
  }
  STAMP(8);

  f32x4 o0 = {0.f, 0.f, 0.f, 0.f}, o1 = {0.f, 0.f, 0.f, 0.f};
  lds_barrier();
  if (has_ffn) {
    *LP(u32x4, L_QA + tid * 32) = w1v;
    *LP(u32x4, L_QA + tid * 32 + 16) = w3v;
    f32x4 fa = {0.f, 0.f, 0.f, 0.f};
    const int rr = r >> 2, cc = r & 3;
#pragma unroll
    for (int k = 0; k < 2; ++k) {
      const int mb = 64 * w + 32 * k + 4 * g;
      const s16x4 lo = __builtin_amdgcn_ds_read_tr16_b64_v4i16(LP(s16x4, L_QA + (mb + rr) * 32 + cc * 8));
      const s16x4 hi = __builtin_amdgcn_ds_read_tr16_b64_v4i16(LP(s16x4, L_QA + (mb + 16 + rr) * 32 + cc * 8));
      const u32x2 l2 = __builtin_bit_cast(u32x2, lo), h2 = __builtin_bit_cast(u32x2, hi);
      const u32x2 xl = *LP(u32x2, L_XN + (r & 1) * 1024 + mb * 2), xh = *LP(u32x2, L_XN + (r & 1) * 1024 + (mb + 16) * 2);
      fa = mfma(u32x4{l2[0], l2[1], h2[0], h2[1]}, u32x4{xl[0], xl[1], xh[0], xh[1]}, fa);
    }
    if (r < 2) *LP(f32x4, L_FP + ((w * 4 + g) * 2 + r) * 16) = fa;
    lds_barrier();
    float gt = 0.f, up = 0.f;
#pragma unroll
    for (int k = 0; k < 8; ++k) {
      gt += *LP(float, L_FP + ((k * 4 + rh) * 2 + bsel) * 16 + g * 4);
      up += *LP(float, L_FP + ((k * 4 + 2 + rh) * 2 + bsel) * 16 + g * 4);
    }
    gt = rbf(gt); up = rbf(up);
    const float sg = rbf(gt / (1.f + __expf(-gt)));
    const u32 hv = pk_bf16(sg * up, 0.f);
    o0 = mfma(w2v, maskfrag(hv, sel_lo), o0);
  }
  STAMP(9);

  {
    const u32 ia = WS_INA + (h * 8 + j) * INA_STRIDE;
    const u32 po = ia + (bsel * 64 + 8 * w + g) * 128 + jj * 16;
    u32x4 pc0, pc1; u32x2 la; u32 l0, l1;
    pc0 = __builtin_amdgcn_raw_buffer_load_b128(wsr, po, 0, 16);
    pc1 = __builtin_amdgcn_raw_buffer_load_b128(wsr, po + 512, 0, 16);
    la = __builtin_amdgcn_raw_buffer_load_b64(wsr, ia + 16384 + lane * 8, 0, 16);
    for (int it = 0; it < SPIN_MAX; ++it) {
      const bool k0 = (pc0[0] & pc0[1] & pc0[2] & pc0[3] & 1u) != 0, k1 = (pc1[0] & pc1[1] & pc1[2] & pc1[3] & 1u) != 0;
      const bool k2 = (la[0] & la[1] & 1u) != 0;
      if (__all(k0 && k1 && k2)) break;
      __builtin_amdgcn_s_sleep(1);
      const u32x4 n0 = __builtin_amdgcn_raw_buffer_load_b128(wsr, k0 ? OOB : po, 0, 16);
      const u32x4 n1 = __builtin_amdgcn_raw_buffer_load_b128(wsr, k1 ? OOB : po + 512, 0, 16);
      const u32x2 n2 = __builtin_amdgcn_raw_buffer_load_b64(wsr, k2 ? OOB : ia + 16384 + lane * 8, 0, 16);
      cfence();
      pc0 = k0 ? pc0 : n0; pc1 = k1 ? pc1 : n1; la = k2 ? la : n2;
    }
    STAMP(10);
    const float e0 = __uint_as_float(la[0]), e1 = __uint_as_float(la[1]);
    const float m0 = wave_max(e0), m1 = wave_max(e1);
    const float a0 = rbf(__builtin_amdgcn_exp2f(e0 - m0)), a1 = rbf(__builtin_amdgcn_exp2f(e1 - m1));
    const float L0 = wave_sum(a0), L1 = wave_sum(a1);
    const u32 ap = pk_bf16(a0, a1);
    l0 = (u32)__builtin_amdgcn_ds_bpermute((8 * w + g) * 4, (int)ap);
    l1 = (u32)__builtin_amdgcn_ds_bpermute((8 * w + 4 + g) * 4, (int)ap);
    const u32x4 sl = bsel ? sel_hi : sel_lo;
    f32x4 d1 = {0.f, 0.f, 0.f, 0.f};
    d1 = mfma(pc0, maskfrag(l0, sl), d1);
    d1 = mfma(pc1, maskfrag(l1, sl), d1);
    if (bsel == (g >> 1)) {
      LAS float* p = LP(float, L_R1 + ((bsel * 64 + 32 * (g & 1) + 16 * (jj >> 2) + (jj & 3)) * 8 + w) * 4);
      p[0] = d1[0]; p[32] = d1[1]; p[64] = d1[2]; p[96] = d1[3];
    }
    lds_barrier();
    const float invL = 1.f / (bsel ? L1 : L0);
    f32x4 d2 = {0.f, 0.f, 0.f, 0.f};
#pragma unroll
    for (int t = 0; t < 2; ++t) {
      const f32x4 u0 = *LP(f32x4, L_R1 + (bsel * 64 + 8 * w + 4 * t + g) * 32);
      const f32x4 u1 = *LP(f32x4, L_R1 + (bsel * 64 + 8 * w + 4 * t + g) * 32 + 16);
      const float v = (((u0[0] + u0[1]) + (u0[2] + u0[3])) + ((u1[0] + u1[1]) + (u1[2] + u1[3]))) * invL;
      d2 = mfma(wuvv[t], maskfrag(pk_bf16(v, v), sel_lo), d2);
    }
    {
      LAS float* p = LP(float, L_R2 + ((bsel * 128 + 32 * g + jj) * 8 + w) * 4);
      p[0] = d2[0]; p[64] = d2[1]; p[128] = d2[2]; p[192] = d2[3];
    }
    lds_barrier();
    {
      const int v0 = (lane >> 5) * 128 + 64 * rh + 2 * (lane & 31);
      const f32x4 u0 = *LP(f32x4, L_R2 + v0 * 32), u1 = *LP(f32x4, L_R2 + v0 * 32 + 16);
      const f32x4 u2 = *LP(f32x4, L_R2 + v0 * 32 + 32), u3 = *LP(f32x4, L_R2 + v0 * 32 + 48);
      const float c0 = ((u0[0] + u0[1]) + (u0[2] + u0[3])) + ((u1[0] + u1[1]) + (u1[2] + u1[3]));
      const float c1 = ((u2[0] + u2[1]) + (u2[2] + u2[3])) + ((u3[0] + u3[1]) + (u3[2] + u3[3]));
      *LP(u32, L_QB + w * 256 + lane * 4) = pk_bf16(c0, c1);
    }
    const u32x4 cf0 = *LP(u32x4, L_QB + w * 256 + bsel * 128 + g * 32), cf1 = *LP(u32x4, L_QB + w * 256 + bsel * 128 + g * 32 + 16);
#pragma unroll
    for (int t = 0; t < 16; t += 2) {
      const u32 d = (t < 8) ? cf0[(t >> 1) & 3] : cf1[(t >> 1) & 3];
      o0 = mfma(wov[t], maskfrag(d, sel_lo), o0);
      o1 = mfma(wov[t + 1], maskfrag(d, sel_hi), o1);
    }
    o0 += o1;
    if (rh == 1) *LP(f32x4, L_R3 + nq * 1024 + lane * 16) = o0;
    lds_barrier();
    if (rh == 0) {
      o0 += *LP(f32x4, L_R3 + nq * 1024 + lane * 16);
      u32x4 o;
#pragma unroll
      for (int k = 0; k < 4; ++k) o[k] = __float_as_uint(o0[k]) | 1u;
      const u32 f = ((bsel * 4 + nq) * 4 + g) * 2 + (jj >> 2);
      __builtin_amdgcn_raw_buffer_store_b128(o, wsr, WS_INB + f * 16384 + bid * 64 + (jj & 3) * 16, 0, 16);
    }
  }
  STAMP(11);

  if (is_final) {
    const int fb = fin >> 5, fq = (fin >> 3) & 3, fg = (fin >> 1) & 3, fa = fin & 1;
    const int n = 128 * fq + 32 * fg + 8 * (tid & 3) + 4 * fa + ((tid >> 2) & 3);
    const float xv = bf_lo((u32)x[fb * 512 + n]);
    const u32 ib = WS_INB + fin * 16384 + (16 * w + (lane >> 2)) * 64 + (lane & 3) * 16;
    u32x4 s0 = __builtin_amdgcn_raw_buffer_load_b128(wsr, ib, 0, 16);
    u32x4 s1 = __builtin_amdgcn_raw_buffer_load_b128(wsr, ib + 8192, 0, 16);
    for (int it = 0; it < SPIN_MAX; ++it) {
      const bool k0 = (s0[0] & s0[1] & s0[2] & s0[3] & 1u) != 0, k1 = (s1[0] & s1[1] & s1[2] & s1[3] & 1u) != 0;
      if (__all(k0 && k1)) break;
      __builtin_amdgcn_s_sleep(1);
      const u32x4 n0 = __builtin_amdgcn_raw_buffer_load_b128(wsr, k0 ? OOB : ib, 0, 16);
      const u32x4 n1 = __builtin_amdgcn_raw_buffer_load_b128(wsr, k1 ? OOB : ib + 8192, 0, 16);
      cfence();
      s0 = k0 ? s0 : n0; s1 = k1 ? s1 : n1;
    }
    STAMP(12);
    f32x4 v;
#pragma unroll
    for (int k = 0; k < 4; ++k) {
      float t = __uint_as_float(s0[k]) + __uint_as_float(s1[k]);
      t += dpp<0x124>(t); t += dpp<0x128>(t);
      v[k] = swap32_add(swap16_add(t));
    }
    if (lane < 4) *LP(f32x4, L_R4 + w * 64 + lane * 16) = v;
    lds_barrier();
    if (tid < 16) {
      float t = 0.f;
#pragma unroll
      for (int k = 0; k < 8; ++k) t += *LP(float, L_R4 + k * 64 + (tid >> 2) * 16 + (tid & 3) * 4);
      out[fb * 512 + n] = (u16)pk_bf16(xv + t, 0.f);
    }
    if (fin == 0 && tid == 0) *(GAS u32*)ws = tag;
    STAMP(13);
  }
  STAMP_FLUSH();
}

extern "C" void kernel_launch(void* const* d_in, const int* in_sizes, int n_in, void* d_out, int out_size, void* d_ws,
                              size_t ws_size, hipStream_t stream) {
  mla_ffn_kernel<<<256, 512, 0, stream>>>((const u16*)d_in[0], (const u16*)d_in[1], (const u16*)d_in[2], (const u16*)d_in[3],
                                          (const u16*)d_in[4], (const u16*)d_in[5], (const u16*)d_in[7], (const u16*)d_in[8],
                                          (const u16*)d_in[9], (u16*)d_out, (unsigned char*)d_ws);
}
```

```cpp
#include <hip/hip_runtime.h>
#include <stdint.h>

typedef unsigned short u16;
typedef unsigned int u32;
typedef unsigned long long u64;
typedef __attribute__((ext_vector_type(4))) float f32x4;
typedef __attribute__((ext_vector_type(8))) short s16x8;
typedef __attribute__((ext_vector_type(4))) short s16x4;
typedef __attribute__((ext_vector_type(4))) u32 u32x4;
typedef __attribute__((ext_vector_type(2))) u32 u32x2;
typedef __attribute__((ext_vector_type(2))) __bf16 bf16x2;
#define GAS __attribute__((address_space(1)))
#define LAS __attribute__((address_space(3)))
#define CAS __attribute__((address_space(4)))

constexpr int SK = 8192, FF = 1376;
constexpr u32 WS_QABS = 4096;
constexpr u32 WS_INA = 1u << 20;
constexpr u32 INA_STRIDE = 16896;
constexpr u32 WS_INB = 8u << 20;
constexpr u32 WS_STAMP = 16u << 20;
constexpr u32 WS_BYTES = 32u << 20;
constexpr u32 L_C = 0;
constexpr u32 L_QA = 131072;
constexpr u32 L_P = 147456;
constexpr u32 L_XN = 151552;
constexpr u32 L_QB = 153600;
constexpr u32 L_MAX = 155648;
constexpr u32 L_FLAG = 156160;
constexpr u32 L_TOTAL = 156224;
constexpr u32 L_R1 = 0;
constexpr u32 L_R2 = 4096;
constexpr u32 L_R3 = 12288;
constexpr u32 L_FP = 16384;
constexpr u32 L_R4 = 17408;

constexpr int SPIN_MAX = 1 << 18;
constexpr u32 OOB = 0x7ffffff0u;

__device__ __forceinline__ float bf_lo(u32 v) { return __uint_as_float(v << 16); }
__device__ __forceinline__ float bf_hi(u32 v) { return __uint_as_float(v & 0xffff0000u); }
__device__ __forceinline__ u32 pk_bf16(float a, float b) {
  bf16x2 v = {(__bf16)a, (__bf16)b};
  return __builtin_bit_cast(u32, v);
}
__device__ __forceinline__ float rbf(float a) { return bf_lo(pk_bf16(a, a)); }
template <int CTRL> __device__ __forceinline__ float dpp(float v) {
  return __int_as_float(__builtin_amdgcn_update_dpp(0, __float_as_int(v), CTRL, 0xf, 0xf, true));
}
__device__ __forceinline__ float swap16_add(float v) {
  auto r = __builtin_amdgcn_permlane16_swap(__float_as_uint(v), __float_as_uint(v), false, false);
  return __uint_as_float(r[0]) + __uint_as_float(r[1]);
}
__device__ __forceinline__ float swap32_add(float v) {
  auto r = __builtin_amdgcn_permlane32_swap(__float_as_uint(v), __float_as_uint(v), false, false);
  return __uint_as_float(r[0]) + __uint_as_float(r[1]);
}
__device__ __forceinline__ float swap16_max(float v) {
  auto r = __builtin_amdgcn_permlane16_swap(__float_as_uint(v), __float_as_uint(v), false, false);
  return fmaxf(__uint_as_float(r[0]), __uint_as_float(r[1]));
}
__device__ __forceinline__ float swap32_max(float v) {
  auto r = __builtin_amdgcn_permlane32_swap(__float_as_uint(v), __float_as_uint(v), false, false);
  return fmaxf(__uint_as_float(r[0]), __uint_as_float(r[1]));
}
__device__ __forceinline__ float wave_sum(float v) {
  v += dpp<0x128>(v); v += dpp<0x124>(v); v += dpp<0x122>(v); v += dpp<0x121>(v);
  return swap32_add(swap16_add(v));
}
__device__ __forceinline__ float wave_max(float v) {
  v = fmaxf(v, dpp<0x128>(v)); v = fmaxf(v, dpp<0x124>(v)); v = fmaxf(v, dpp<0x122>(v)); v = fmaxf(v, dpp<0x121>(v));
  return swap32_max(swap16_max(v));
}
__device__ __forceinline__ f32x4 mfma(u32x4 a, u32x4 b, f32x4 c) {
  return __builtin_amdgcn_mfma_f32_16x16x32_bf16(__builtin_bit_cast(s16x8, a), __builtin_bit_cast(s16x8, b), c, 0, 0, 0);
}
__device__ __forceinline__ void lds_barrier() { asm volatile("s_waitcnt lgkmcnt(0)\n\ts_barrier" ::: "memory"); }
__device__ __forceinline__ void cfence() { asm volatile("" ::: "memory"); __builtin_amdgcn_sched_barrier(0); }

#define LP(T, off) ((LAS T*)(lds + (off)))
#define LDW(p) (*(const GAS u32x4*)(p))

__device__ __forceinline__ void flag_set(u32 addr, u32 tag) {
  asm volatile("ds_write_b32 %0, %1" ::"v"(addr), "v"(tag) : "memory");
}
__device__ __forceinline__ void flag_wait4(u32 addr, u32 tag) {
  for (int it = 0; it < SPIN_MAX; ++it) {
    u32x4 v;
    asm volatile("ds_read_b128 %0, %1\n\ts_waitcnt lgkmcnt(0)" : "=v"(v) : "v"(addr) : "memory");
    if (v[0] == tag && v[1] == tag && v[2] == tag && v[3] == tag) break;
    __builtin_amdgcn_s_sleep(1);
  }
}
__device__ __forceinline__ u32x4 maskfrag(u32 d, const u32x4& sel) {
  u32x4 f;
  f[0] = __builtin_amdgcn_perm(d, d, sel[0]); f[1] = __builtin_amdgcn_perm(d, d, sel[1]);
  f[2] = __builtin_amdgcn_perm(d, d, sel[2]); f[3] = __builtin_amdgcn_perm(d, d, sel[3]);
  return f;
}

#define STAMP(k) do {} while (0)
#define STAMP_DECL do {} while (0)
#define STAMP_FLUSH() do {} while (0)

__global__ __launch_bounds__(512, 2) void mla_ffn_kernel(
    const u16* __restrict__ x, const u16* __restrict__ ckv, const u16* __restrict__ wq, const u16* __restrict__ wuk,
    const u16* __restrict__ wuv, const u16* __restrict__ wo, const u16* __restrict__ w1, const u16* __restrict__ w3,
    const u16* __restrict__ w2, u16* __restrict__ out, unsigned char* __restrict__ ws) {
  __shared__ __attribute__((aligned(16))) unsigned char lds_raw[L_TOTAL];
  LAS unsigned char* lds = (LAS unsigned char*)lds_raw;
  const u32 ldsb = (u32)(uintptr_t)lds;
  const int tid = threadIdx.x, lane = tid & 63;
  const int w = __builtin_amdgcn_readfirstlane(tid >> 6);
  const int bid = blockIdx.x, X = bid & 7, I = bid >> 3;
  const int h = 4 * X + (I >> 3), j = I & 7;
  const int pair = 16 * X + (I >> 1), b = pair >> 6, s = pair & 63, G = I & 1;
  const bool is_final = (I < 16) && ((I & 1) != (X >> 2));
  const int fin = X * 8 + (I >> 1);
  const int fu = 22 * X + (I - 10);
  const bool has_ffn = (I >= 10) && (fu < 172);
  const int g = lane >> 4, r = lane & 15, jj = lane & 7, bsel = (lane >> 3) & 1;
  STAMP_DECL;
  STAMP(0);
  u32 tag;
  asm volatile("" ::"s"(wuv), "s"(wo), "s"(w1), "s"(w3), "s"(w2), "s"(out), "s"(x), "s"(ckv), "s"(wq), "s"(wuk));
  const __amdgpu_buffer_rsrc_t w1r = __builtin_amdgcn_make_buffer_rsrc((void*)w1, 0, 512 * FF * 2, 0x00020000);
  const __amdgpu_buffer_rsrc_t w3r = __builtin_amdgcn_make_buffer_rsrc((void*)w3, 0, 512 * FF * 2, 0x00020000);
  const __amdgpu_buffer_rsrc_t w2r = __builtin_amdgcn_make_buffer_rsrc((void*)w2, 0, 512 * FF * 2, 0x00020000);
  const __amdgpu_buffer_rsrc_t wsr = __builtin_amdgcn_make_buffer_rsrc((void*)ws, 0, WS_BYTES, 0x00020000);

  const __amdgpu_buffer_rsrc_t wqr = __builtin_amdgcn_make_buffer_rsrc((void*)wq, 0, 512 * 4096 * 2, 0x00020000);
  const __amdgpu_buffer_rsrc_t wor = __builtin_amdgcn_make_buffer_rsrc((void*)wo, 0, 512 * 4096 * 2, 0x00020000);
  u32x4 sel_lo, sel_hi;
#pragma unroll
  for (int k = 0; k < 4; ++k) {
    const bool on = (k == (jj >> 1));
    sel_lo[k] = on ? ((jj & 1) ? 0x01000c0cu : 0x0c0c0100u) : 0x0c0c0c0cu;
    sel_hi[k] = on ? ((jj & 1) ? 0x03020c0cu : 0x0c0c0302u) : 0x0c0c0c0cu;
  }

  u32 touch = 0;
  if (w < 4) {
    u32 ep;
    asm volatile("s_load_dword %0, %1, 0x0" : "=s"(ep) : "s"(ws));
    const u32x4 x0 = LDW(x + 8 * lane), x1 = LDW(x + 512 + 8 * lane);
    u32x4 wqv[32], wukv[4];
    {
      const u32 vo = ((128 * w + 32 * g) * 4096 + h * 128 + r * 8) * 2;
#pragma unroll
      for (int t = 0; t < 32; ++t) wqv[t] = __builtin_amdgcn_raw_buffer_load_b128(wqr, vo, t * 8192, 0);
      const u16* pk = wuk + (size_t)(64 * j + 16 * w + r) * 4096 + h * 128 + g * 8;
#pragma unroll
      for (int k = 0; k < 4; ++k) wukv[k] = LDW(pk + k * 32);
    }
    cfence();
    asm volatile("s_barrier" ::: "memory");
    asm volatile("s_waitcnt lgkmcnt(0)" : "+s"(ep));
    tag = ep + 1u;
    STAMP(1);
    {
      float a[8], c[8];
#pragma unroll
      for (int k = 0; k < 4; ++k) { a[2 * k] = bf_lo(x0[k]); a[2 * k + 1] = bf_hi(x0[k]); c[2 * k] = bf_lo(x1[k]); c[2 * k + 1] = bf_hi(x1[k]); }
      float sa = ((a[0] + a[1]) + (a[2] + a[3])) + ((a[4] + a[5]) + (a[6] + a[7]));
      float sc = ((c[0] + c[1]) + (c[2] + c[3])) + ((c[4] + c[5]) + (c[6] + c[7]));
      sa = wave_sum(sa) * (1.f / 512.f); sc = wave_sum(sc) * (1.f / 512.f);
      float va = 0.f, vc = 0.f, vb = 0.f, vd = 0.f;
#pragma unroll
      for (int k = 0; k < 8; k += 2) {
        const float da = a[k] - sa, dc = c[k] - sc, db = a[k + 1] - sa, dd = c[k + 1] - sc;
        va += da * da; vc += dc * dc; vb += db * db; vd += dd * dd;
      }
      va = wave_sum(va + vb) * (1.f / 512.f); vc = wave_sum(vc + vd) * (1.f / 512.f);
      const float ma = rbf(sa), mc = rbf(sc);
      const float ra = __builtin_amdgcn_rcpf(rbf(__builtin_amdgcn_sqrtf(rbf(rbf(va) + 1e-5f))));
      const float rc = __builtin_amdgcn_rcpf(rbf(__builtin_amdgcn_sqrtf(rbf(rbf(vc) + 1e-5f))));
      u32x4 n0, n1;
#pragma unroll
      for (int k = 0; k < 4; ++k) {
        const u32 da = pk_bf16(a[2 * k] - ma, a[2 * k + 1] - ma), dc = pk_bf16(c[2 * k] - mc, c[2 * k + 1] - mc);
        n0[k] = pk_bf16(bf_lo(da) * ra, bf_hi(da) * ra);
        n1[k] = pk_bf16(bf_lo(dc) * rc, bf_hi(dc) * rc);
      }
      *LP(u32x4, L_XN + 16 * lane) = n0;
      *LP(u32x4, L_XN + 1024 + 16 * lane) = n1;
    }
    STAMP(2);
    u32x4 xf[4];
#pragma unroll
    for (int u = 0; u < 4; ++u) xf[u] = *LP(u32x4, L_XN + bsel * 1024 + (128 * w + 32 * g) * 2 + 16 * u);
    f32x4 q0 = {0.f, 0.f, 0.f, 0.f}, q1 = {0.f, 0.f, 0.f, 0.f};
#pragma unroll
    for (int t = 0; t < 32; t += 2) {
      const u32 d = xf[t >> 3][(t >> 1) & 3];
      q0 = mfma(wqv[t], maskfrag(d, sel_lo), q0);
      q1 = mfma(wqv[t + 1], maskfrag(d, sel_hi), q1);
    }
    q0 += q1;
    *LP(f32x4, L_P + w * 1024 + 16 * lane) = q0;
    flag_set(ldsb + L_FLAG + 4 * w, tag);
    STAMP(3);
    flag_wait4(ldsb + L_FLAG, tag);
    {
      f32x4 q = *LP(f32x4, L_P + 16 * lane);
      q += *LP(f32x4, L_P + 1024 + 16 * lane);
      q += *LP(f32x4, L_P + 2048 + 16 * lane);
      q += *LP(f32x4, L_P + 3072 + 16 * lane);
      const u32 p01 = pk_bf16(q[0], q[1]), p23 = pk_bf16(q[2], q[3]);
      LAS u16* qb = LP(u16, L_QB + w * 512 + bsel * 256 + (32 * g + jj) * 2);
      qb[0] = (u16)p01; qb[8] = (u16)(p01 >> 16); qb[16] = (u16)p23; qb[24] = (u16)(p23 >> 16);
    }
    f32x4 qa = {0.f, 0.f, 0.f, 0.f};
#pragma unroll
    for (int k = 0; k < 4; ++k) {
      const u32x4 bq = *LP(u32x4, L_QB + w * 512 + (r & 1) * 256 + (32 * k + 8 * g) * 2);
      qa = mfma(wukv[k], bq, qa);
    }
    const float qs = 0.08838834764831845f * 1.4426950408889634f;
    u32x4 gr;
    gr[0] = pk_bf16(qa[0] * qs, qa[1] * qs); gr[1] = tag; gr[2] = pk_bf16(qa[2] * qs, qa[3] * qs); gr[3] = tag;
    flag_wait4(ldsb + L_FLAG + 16, tag);
    if (r < 2)
      __builtin_amdgcn_raw_buffer_store_b128(gr, wsr, WS_QABS + ((r * 32 + h) * 256 + 32 * j + 8 * w + 2 * g) * 8, 0, 16);
    STAMP(4);
  } else {
    const int t = tid - 256;
    const u32x4 z = {0u, 0u, 0u, 0u};
    const u32 ia = WS_INA + (h * 8 + j) * INA_STRIDE + t * 16;
#pragma unroll
    for (int k = 0; k < 4; ++k) __builtin_amdgcn_raw_buffer_store_b128(z, wsr, ia + k * 4096, 0, 16);
    if (t < 32) __builtin_amdgcn_raw_buffer_store_b128(z, wsr, ia + 16384, 0, 16);
    if (is_final) {
      const u32 ib = WS_INB + fin * 16384 + t * 16;
#pragma unroll
      for (int k = 0; k < 4; ++k) __builtin_amdgcn_raw_buffer_store_b128(z, wsr, ib + k * 4096, 0, 16);
    }
    u32 ep;
    asm volatile("s_load_dword %0, %1, 0x0" : "=s"(ep) : "s"(ws));
    cfence();
    asm volatile("s_barrier" ::: "memory");
    asm volatile("s_waitcnt lgkmcnt(0)" : "+s"(ep));
    tag = ep + 1u;
    const int u = w - 4;
    const u16* cbase = ckv + ((size_t)b * SK + s * 128 + 32 * u) * 512;
#pragma unroll
    for (int i = 0; i < 32; ++i) {
      const u32 voff = i * 1024 + 16 * (lane ^ (2 * (i & 7)));
      const u32 dst = ldsb + L_C + (32 * u + i) * 1024;
      asm volatile("s_mov_b32 m0, %2\n\ts_nop 0\n\tglobal_load_lds_dwordx4 %0, %1" ::"v"(voff), "s"(cbase), "s"(dst) : "memory");
      if (i == 7) {
        asm volatile("s_waitcnt vmcnt(8)" ::: "memory");
        flag_set(ldsb + L_FLAG + 16 + 4 * u, tag);
      }
    }
    STAMP(1);
    if (u < 2) {
      const u32 to = ((h * 128 + 16 * j) * 512) * 2 + (u * 64 + lane) * 128;
      asm volatile("global_load_dword %0, %1, %2" : "=v"(touch) : "v"(to), "s"(wo) : "memory");
    }
    STAMP(2);
    flag_wait4(ldsb + L_FLAG + 32, tag);
    STAMP(3);
  }

  u32x4 gq[4];
  const u32 gqo = WS_QABS + ((b * 32 + 16 * G + 2 * w) * 256 + 2 * lane) * 8;
#pragma unroll
  for (int u = 0; u < 4; ++u) gq[u] = __builtin_amdgcn_raw_buffer_load_b128(wsr, gqo + (u >> 1) * 2048 + (u & 1) * 1024, 0, 16);
  cfence();
  if (w < 4) flag_set(ldsb + L_FLAG + 32 + 4 * w, tag);
  u32x4 wuvv[2], wov[16], w1v, w3v, w2v;
  const int nq = w & 3, rh = w >> 2;
  {
    const u16* pv = wuv + (size_t)(64 * j + 8 * w + g) * 4096 + h * 128 + r * 8;
    wuvv[0] = LDW(pv); wuvv[1] = LDW(pv + 4 * 4096);
    w1v = __builtin_amdgcn_raw_buffer_load_b128(w1r, has_ffn ? (u32)(tid * FF + 8 * fu) * 2u : OOB, 0, 0);
    w3v = __builtin_amdgcn_raw_buffer_load_b128(w3r, has_ffn ? (u32)(tid * FF + 8 * fu) * 2u : OOB, 0, 0);
    w2v = __builtin_amdgcn_raw_buffer_load_b128(w2r, has_ffn ? (u32)((8 * fu + 4 * rh + g) * 512 + 128 * nq + 8 * r) * 2u : OOB, 0, 0);
  }
  cfence();
  for (int it = 0; it < SPIN_MAX; ++it) {
    bool k[4];
#pragma unroll
    for (int u = 0; u < 4; ++u) k[u] = (gq[u][1] == tag) & (gq[u][3] == tag);
    if (__all(k[0] & k[1] & k[2] & k[3])) break;
    __builtin_amdgcn_s_sleep(1);
    u32x4 nw[4];
#pragma unroll
    for (int u = 0; u < 4; ++u) nw[u] = __builtin_amdgcn_raw_buffer_load_b128(wsr, k[u] ? OOB : gqo + (u >> 1) * 2048 + (u & 1) * 1024, 0, 16);
    cfence();
#pragma unroll
    for (int u = 0; u < 4; ++u) gq[u] = k[u] ? gq[u] : nw[u];
  }
  asm volatile("" ::"v"(touch));
  STAMP(5);
#pragma unroll
  for (int u = 0; u < 4; ++u) {
    const int hl = 2 * w + (u >> 1), idx = (u & 1) * 64 + lane;
    *LP(u32x2, L_QA + hl * 1024 + 16 * ((idx >> 1) ^ (2 * (hl & 7))) + 8 * (idx & 1)) = u32x2{gq[u][0], gq[u][2]};
  }
  u32 qk_a[4], qk_b[4], pv_a[4];
#pragma unroll
  for (int k = 0; k < 4; ++k) {
    const u32 o = 16 * ((4 * k + g) ^ (2 * (r & 7)));
    qk_a[k] = L_C + (16 * w + r) * 1024 + o;
    qk_b[k] = L_QA + r * 1024 + o;
    pv_a[k] = L_C + (4 * g + (r >> 2)) * 1024 + 16 * ((8 * w + 2 * k + ((r & 3) >> 1)) ^ (2 * (4 * (g & 1) + (r >> 2)))) + 8 * (r & 1);
  }
  lds_barrier();
  STAMP(6);

  float pm, pl;
  f32x4 sc;
  {
    f32x4 s0 = {0.f, 0.f, 0.f, 0.f}, s1 = {0.f, 0.f, 0.f, 0.f};
    u32x4 av[16], bv[16];
#pragma unroll
    for (int kk = 0; kk < 16; ++kk) {
      av[kk] = *LP(u32x4, qk_a[kk & 3] + (kk >> 2) * 256);
      bv[kk] = *LP(u32x4, qk_b[kk & 3] + (kk >> 2) * 256);
    }
    __builtin_amdgcn_sched_barrier(0);
#pragma unroll
    for (int kk = 0; kk < 16; kk += 2) {
      s0 = mfma(av[kk], bv[kk], s0);
      s1 = mfma(av[kk + 1], bv[kk + 1], s1);
    }
    sc = s0 + s1;
    float m = fmaxf(fmaxf(sc[0], sc[1]), fmaxf(sc[2], sc[3]));
    m = swap32_max(swap16_max(m));
    if (g == 0) *LP(float, L_MAX + (r * 8 + w) * 4) = m;
  }
  lds_barrier();
  {
    const f32x4 m0 = *LP(f32x4, L_MAX + r * 32), m1 = *LP(f32x4, L_MAX + r * 32 + 16);
    pm = fmaxf(fmaxf(fmaxf(m0[0], m0[1]), fmaxf(m0[2], m0[3])), fmaxf(fmaxf(m1[0], m1[1]), fmaxf(m1[2], m1[3])));
    const float p0 = __builtin_amdgcn_exp2f(sc[0] - pm), p1 = __builtin_amdgcn_exp2f(sc[1] - pm);
    const float p2 = __builtin_amdgcn_exp2f(sc[2] - pm), p3 = __builtin_amdgcn_exp2f(sc[3] - pm);
    *LP(u32x2, L_P + (w >> 1) * 1024 + lane * 16 + (w & 1) * 8) = u32x2{pk_bf16(p0, p1), pk_bf16(p2, p3)};
  }
  lds_barrier();
  STAMP(7);
  {
    u32x4 pf[4];
#pragma unroll
    for (int ks = 0; ks < 4; ++ks) pf[ks] = *LP(u32x4, L_P + ks * 1024 + lane * 16);
    f32x4 acc[4], accl = {0.f, 0.f, 0.f, 0.f};
    const u32x4 ones = {0x3f803f80u, 0x3f803f80u, 0x3f803f80u, 0x3f803f80u};
    s16x4 lo[16], hi[16];
#pragma unroll
    for (int ks = 0; ks < 4; ++ks)
#pragma unroll
      for (int ct = 0; ct < 4; ++ct) {
        lo[ks * 4 + ct] = __builtin_amdgcn_ds_read_tr16_b64_v4i16(LP(s16x4, pv_a[ct] + ks * 32768));
        hi[ks * 4 + ct] = __builtin_amdgcn_ds_read_tr16_b64_v4i16(LP(s16x4, pv_a[ct] + ks * 32768 + 16384));
      }
    __builtin_amdgcn_sched_barrier(0);
#pragma unroll
    for (int ct = 0; ct < 4; ++ct) acc[ct] = f32x4{0.f, 0.f, 0.f, 0.f};
#pragma unroll
    for (int ks = 0; ks < 4; ++ks)
#pragma unroll
      for (int ct = 0; ct < 4; ++ct) {
        const u32x2 l2 = __builtin_bit_cast(u32x2, lo[ks * 4 + ct]), h2 = __builtin_bit_cast(u32x2, hi[ks * 4 + ct]);
        acc[ct] = mfma(u32x4{l2[0], l2[1], h2[0], h2[1]}, pf[ks], acc[ct]);
      }
#pragma unroll
    for (int ks = 0; ks < 4; ++ks) accl = mfma(ones, pf[ks], accl);
    pl = accl[0];
    const float inv = __builtin_amdgcn_rcpf(pl);
    const u32 dst = WS_INA + ((16 * G + r) * 8 + w) * INA_STRIDE;
#pragma unroll
    for (int hf = 0; hf < 2; ++hf) {
      u32x4 o;
      o[0] = pk_bf16(acc[2 * hf][0] * inv, acc[2 * hf][1] * inv) | 1u;
      o[1] = pk_bf16(acc[2 * hf][2] * inv, acc[2 * hf][3] * inv) | 1u;
      o[2] = pk_bf16(acc[2 * hf + 1][0] * inv, acc[2 * hf + 1][1] * inv) | 1u;
      o[3] = pk_bf16(acc[2 * hf + 1][2] * inv, acc[2 * hf + 1][3] * inv) | 1u;
      __builtin_amdgcn_raw_buffer_store_b128(o, wsr, dst + (b * 64 + s) * 128 + hf * 64 + g * 16, 0, 16);
    }
    if (g == 0) {
      const float lse = pm + __builtin_amdgcn_logf(pl);
      __builtin_amdgcn_raw_buffer_store_b32(__float_as_uint(lse) | 1u, wsr, dst + 16384 + (s * 2 + b) * 4, 0, 16);
    }
  }
  STAMP(8);
  cfence();
  {
    const u32 vo = ((h * 128 + 64 * rh + 16 * g) * 512 + 128 * nq + 8 * r) * 2;
#pragma unroll
    for (int t = 0; t < 16; ++t) wov[t] = __builtin_amdgcn_raw_buffer_load_b128(wor, vo, t * 1024, 0);
  }
  cfence();

  f32x4 o0 = {0.f, 0.f, 0.f, 0.f}, o1 = {0.f, 0.f, 0.f, 0.f};
  lds_barrier();
  if (has_ffn) {
    *LP(u32x4, L_QA + tid * 32) = w1v;
    *LP(u32x4, L_QA + tid * 32 + 16) = w3v;
    f32x4 fa = {0.f, 0.f, 0.f, 0.f};
    const int rr = r >> 2, cc = r & 3;
#pragma unroll
    for (int k = 0; k < 2; ++k) {
      const int mb = 64 * w + 32 * k + 4 * g;
      const s16x4 lo = __builtin_amdgcn_ds_read_tr16_b64_v4i16(LP(s16x4, L_QA + (mb + rr) * 32 + cc * 8));
      const s16x4 hi = __builtin_amdgcn_ds_read_tr16_b64_v4i16(LP(s16x4, L_QA + (mb + 16 + rr) * 32 + cc * 8));
      const u32x2 l2 = __builtin_bit_cast(u32x2, lo), h2 = __builtin_bit_cast(u32x2, hi);
      const u32x2 xl = *LP(u32x2, L_XN + (r & 1) * 1024 + mb * 2), xh = *LP(u32x2, L_XN + (r & 1) * 1024 + (mb + 16) * 2);
      fa = mfma(u32x4{l2[0], l2[1], h2[0], h2[1]}, u32x4{xl[0], xl[1], xh[0], xh[1]}, fa);
    }
    if (r < 2) *LP(f32x4, L_FP + ((w * 4 + g) * 2 + r) * 16) = fa;
    lds_barrier();
    float gt = 0.f, up = 0.f;
#pragma unroll
    for (int k = 0; k < 8; ++k) {
      gt += *LP(float, L_FP + ((k * 4 + rh) * 2 + bsel) * 16 + g * 4);
      up += *LP(float, L_FP + ((k * 4 + 2 + rh) * 2 + bsel) * 16 + g * 4);
    }
    gt = rbf(gt); up = rbf(up);
    const float sg = rbf(gt * __builtin_amdgcn_rcpf(1.f + __expf(-gt)));
    const u32 hv = pk_bf16(sg * up, 0.f);
    o0 = mfma(w2v, maskfrag(hv, sel_lo), o0);
  }
  STAMP(9);

  {
    const u32 ia = WS_INA + (h * 8 + j) * INA_STRIDE;
    const u32 po = ia + (bsel * 64 + 8 * w + g) * 128 + jj * 16;
    u32x4 pc0, pc1; u32x2 la; u32 l0, l1;
    pc0 = __builtin_amdgcn_raw_buffer_load_b128(wsr, po, 0, 16);
    pc1 = __builtin_amdgcn_raw_buffer_load_b128(wsr, po + 512, 0, 16);
    la = __builtin_amdgcn_raw_buffer_load_b64(wsr, ia + 16384 + lane * 8, 0, 16);
    for (int it = 0; it < SPIN_MAX; ++it) {
      const bool k0 = (pc0[0] & pc0[1] & pc0[2] & pc0[3] & 1u) != 0, k1 = (pc1[0] & pc1[1] & pc1[2] & pc1[3] & 1u) != 0;
      const bool k2 = (la[0] & la[1] & 1u) != 0;
      if (__all(k0 && k1 && k2)) break;
      __builtin_amdgcn_s_sleep(1);
      const u32x4 n0 = __builtin_amdgcn_raw_buffer_load_b128(wsr, k0 ? OOB : po, 0, 16);
      const u32x4 n1 = __builtin_amdgcn_raw_buffer_load_b128(wsr, k1 ? OOB : po + 512, 0, 16);
      const u32x2 n2 = __builtin_amdgcn_raw_buffer_load_b64(wsr, k2 ? OOB : ia + 16384 + lane * 8, 0, 16);
      cfence();
      pc0 = k0 ? pc0 : n0; pc1 = k1 ? pc1 : n1; la = k2 ? la : n2;
    }
    STAMP(10);
    const float e0 = __uint_as_float(la[0]), e1 = __uint_as_float(la[1]);
    const float m0 = wave_max(e0), m1 = wave_max(e1);
    const float a0 = rbf(__builtin_amdgcn_exp2f(e0 - m0)), a1 = rbf(__builtin_amdgcn_exp2f(e1 - m1));
    const float L0 = wave_sum(a0), L1 = wave_sum(a1);
    const u32 ap = pk_bf16(a0, a1);
    l0 = (u32)__builtin_amdgcn_ds_bpermute((8 * w + g) * 4, (int)ap);
    l1 = (u32)__builtin_amdgcn_ds_bpermute((8 * w + 4 + g) * 4, (int)ap);
    const u32x4 sl = bsel ? sel_hi : sel_lo;
    f32x4 d1 = {0.f, 0.f, 0.f, 0.f};
    d1 = mfma(pc0, maskfrag(l0, sl), d1);
    d1 = mfma(pc1, maskfrag(l1, sl), d1);
    if (bsel == (g >> 1)) {
      LAS float* p = LP(float, L_R1 + ((bsel * 64 + 32 * (g & 1) + 16 * (jj >> 2) + (jj & 3)) * 8 + w) * 4);
      p[0] = d1[0]; p[32] = d1[1]; p[64] = d1[2]; p[96] = d1[3];
    }
    lds_barrier();
    const float invL = __builtin_amdgcn_rcpf(bsel ? L1 : L0);
    f32x4 d2 = {0.f, 0.f, 0.f, 0.f};
#pragma unroll
    for (int t = 0; t < 2; ++t) {
      const f32x4 u0 = *LP(f32x4, L_R1 + (bsel * 64 + 8 * w + 4 * t + g) * 32);
      const f32x4 u1 = *LP(f32x4, L_R1 + (bsel * 64 + 8 * w + 4 * t + g) * 32 + 16);
      const float v = (((u0[0] + u0[1]) + (u0[2] + u0[3])) + ((u1[0] + u1[1]) + (u1[2] + u1[3]))) * invL;
      d2 = mfma(wuvv[t], maskfrag(pk_bf16(v, v), sel_lo), d2);
    }
    {
      LAS float* p = LP(float, L_R2 + ((bsel * 128 + 32 * g + jj) * 8 + w) * 4);
      p[0] = d2[0]; p[64] = d2[1]; p[128] = d2[2]; p[192] = d2[3];
    }
    lds_barrier();
    {
      const int v0 = (lane >> 5) * 128 + 64 * rh + 2 * (lane & 31);
      const f32x4 u0 = *LP(f32x4, L_R2 + v0 * 32), u1 = *LP(f32x4, L_R2 + v0 * 32 + 16);
      const f32x4 u2 = *LP(f32x4, L_R2 + v0 * 32 + 32), u3 = *LP(f32x4, L_R2 + v0 * 32 + 48);
      const float c0 = ((u0[0] + u0[1]) + (u0[2] + u0[3])) + ((u1[0] + u1[1]) + (u1[2] + u1[3]));
      const float c1 = ((u2[0] + u2[1]) + (u2[2] + u2[3])) + ((u3[0] + u3[1]) + (u3[2] + u3[3]));
      *LP(u32, L_QB + w * 256 + lane * 4) = pk_bf16(c0, c1);
    }
    const u32x4 cf0 = *LP(u32x4, L_QB + w * 256 + bsel * 128 + g * 32), cf1 = *LP(u32x4, L_QB + w * 256 + bsel * 128 + g * 32 + 16);
#pragma unroll
    for (int t = 0; t < 16; t += 2) {
      const u32 d = (t < 8) ? cf0[(t >> 1) & 3] : cf1[(t >> 1) & 3];
      o0 = mfma(wov[t], maskfrag(d, sel_lo), o0);
      o1 = mfma(wov[t + 1], maskfrag(d, sel_hi), o1);
    }
    o0 += o1;
    if (rh == 1) *LP(f32x4, L_R3 + nq * 1024 + lane * 16) = o0;
    lds_barrier();
    if (rh == 0) {
      o0 += *LP(f32x4, L_R3 + nq * 1024 + lane * 16);
      u32x4 o;
#pragma unroll
      for (int k = 0; k < 4; ++k) o[k] = __float_as_uint(o0[k]) | 1u;
      const u32 f = ((bsel * 4 + nq) * 4 + g) * 2 + (jj >> 2);
      __builtin_amdgcn_raw_buffer_store_b128(o, wsr, WS_INB + f * 16384 + bid * 64 + (jj & 3) * 16, 0, 16);
    }
  }
  STAMP(11);

  if (is_final) {
    const int fb = fin >> 5, fq = (fin >> 3) & 3, fg = (fin >> 1) & 3, fa = fin & 1;
    const int n = 128 * fq + 32 * fg + 8 * (tid & 3) + 4 * fa + ((tid >> 2) & 3);
    const float xv = bf_lo((u32)x[fb * 512 + n]);
    const u32 ib = WS_INB + fin * 16384 + (16 * w + (lane >> 2)) * 64 + (lane & 3) * 16;
    u32x4 s0 = __builtin_amdgcn_raw_buffer_load_b128(wsr, ib, 0, 16);
    u32x4 s1 = __builtin_amdgcn_raw_buffer_load_b128(wsr, ib + 8192, 0, 16);
    for (int it = 0; it < SPIN_MAX; ++it) {
      const bool k0 = (s0[0] & s0[1] & s0[2] & s0[3] & 1u) != 0, k1 = (s1[0] & s1[1] & s1[2] & s1[3] & 1u) != 0;
      if (__all(k0 && k1)) break;
      __builtin_amdgcn_s_sleep(1);
      const u32x4 n0 = __builtin_amdgcn_raw_buffer_load_b128(wsr, k0 ? OOB : ib, 0, 16);
      const u32x4 n1 = __builtin_amdgcn_raw_buffer_load_b128(wsr, k1 ? OOB : ib + 8192, 0, 16);
      cfence();
      s0 = k0 ? s0 : n0; s1 = k1 ? s1 : n1;
    }
    STAMP(12);
    f32x4 v;
#pragma unroll
    for (int k = 0; k < 4; ++k) {
      float t = __uint_as_float(s0[k]) + __uint_as_float(s1[k]);
      t += dpp<0x124>(t); t += dpp<0x128>(t);
      v[k] = swap32_add(swap16_add(t));
    }
    if (lane < 4) *LP(f32x4, L_R4 + w * 64 + lane * 16) = v;
    lds_barrier();
    if (tid < 16) {
      float t = 0.f;
#pragma unroll
      for (int k = 0; k < 8; ++k) t += *LP(float, L_R4 + k * 64 + (tid >> 2) * 16 + (tid & 3) * 4);
      out[fb * 512 + n] = (u16)pk_bf16(xv + t, 0.f);
    }
    if (fin == 0 && tid == 0) *(GAS u32*)ws = tag;
    STAMP(13);
  }
  STAMP_FLUSH();
}

extern "C" void kernel_launch(void* const* d_in, const int* in_sizes, int n_in, void* d_out, int out_size, void* d_ws,
                              size_t ws_size, hipStream_t stream) {
  mla_ffn_kernel<<<256, 512, 0, stream>>>((const u16*)d_in[0], (const u16*)d_in[1], (const u16*)d_in[2], (const u16*)d_in[3],
                                          (const u16*)d_in[4], (const u16*)d_in[5], (const u16*)d_in[7], (const u16*)d_in[8],
                                          (const u16*)d_in[9], (u16*)d_out, (unsigned char*)d_ws);
}
```

```cpp
#include <hip/hip_runtime.h>
#include <stdint.h>

typedef unsigned short u16;
typedef unsigned int u32;
typedef unsigned long long u64;
typedef __attribute__((ext_vector_type(4))) float f32x4;
typedef __attribute__((ext_vector_type(8))) short s16x8;
typedef __attribute__((ext_vector_type(4))) short s16x4;
typedef __attribute__((ext_vector_type(4))) u32 u32x4;
typedef __attribute__((ext_vector_type(2))) u32 u32x2;
typedef __attribute__((ext_vector_type(2))) __bf16 bf16x2;
#define GAS __attribute__((address_space(1)))
#define LAS __attribute__((address_space(3)))
#define CAS __attribute__((address_space(4)))

constexpr int SK = 8192, FF = 1376;
constexpr u32 WS_QABS = 4096;
constexpr u32 WS_READY = 2048;
constexpr u32 WS_READY2 = 3072;
constexpr u32 WS_LSE = 512u << 10;
constexpr u32 WS_INF = 144u << 10;
constexpr u32 WS_INA = 1u << 20;
constexpr u32 INA_STRIDE = 16384;
constexpr u32 WS_INB = 8u << 20;
constexpr u32 WS_BYTES = 32u << 20;
constexpr u32 L_C = 0;
constexpr u32 L_QA = 131072;
constexpr u32 L_P = 147456;
constexpr u32 L_XN = 151552;
constexpr u32 L_QB = 153600;
constexpr u32 L_MAX = 155648;
constexpr u32 L_FLAG = 156160;
constexpr u32 L_F1 = 156352;
constexpr u32 L_TOTAL = 160448;
constexpr u32 F_Q = L_FLAG, F_G = L_FLAG + 16, F_M = L_FLAG + 32, F_P = L_FLAG + 48, F_C3 = L_FLAG + 64, F_C4 = L_FLAG + 80,
              F_F = L_FLAG + 96, F_Z = L_FLAG + 112, F_XN = L_FLAG + 128, F_PV = L_FLAG + 144, F_L = L_FLAG + 160;
constexpr u32 L_R1 = 0;
constexpr u32 L_R2 = 4096;
constexpr u32 L_R4 = 17408;

constexpr int SPIN_MAX = 1 << 18;
constexpr u32 OOB = 0x7ffffff0u;

__device__ __forceinline__ float bf_lo(u32 v) { return __uint_as_float(v << 16); }
__device__ __forceinline__ float bf_hi(u32 v) { return __uint_as_float(v & 0xffff0000u); }
__device__ __forceinline__ u32 pk_bf16(float a, float b) {
  bf16x2 v = {(__bf16)a, (__bf16)b};
  return __builtin_bit_cast(u32, v);
}
__device__ __forceinline__ float rbf(float a) { return bf_lo(pk_bf16(a, a)); }
template <int CTRL> __device__ __forceinline__ float dpp(float v) {
  return __int_as_float(__builtin_amdgcn_update_dpp(0, __float_as_int(v), CTRL, 0xf, 0xf, true));
}
__device__ __forceinline__ float swap16_add(float v) {
  auto r = __builtin_amdgcn_permlane16_swap(__float_as_uint(v), __float_as_uint(v), false, false);
  return __uint_as_float(r[0]) + __uint_as_float(r[1]);
}
__device__ __forceinline__ float swap32_add(float v) {
  auto r = __builtin_amdgcn_permlane32_swap(__float_as_uint(v), __float_as_uint(v), false, false);
  return __uint_as_float(r[0]) + __uint_as_float(r[1]);
}
__device__ __forceinline__ float swap16_max(float v) {
  auto r = __builtin_amdgcn_permlane16_swap(__float_as_uint(v), __float_as_uint(v), false, false);
  return fmaxf(__uint_as_float(r[0]), __uint_as_float(r[1]));
}
__device__ __forceinline__ float swap32_max(float v) {
  auto r = __builtin_amdgcn_permlane32_swap(__float_as_uint(v), __float_as_uint(v), false, false);
  return fmaxf(__uint_as_float(r[0]), __uint_as_float(r[1]));
}
__device__ __forceinline__ float wave_sum(float v) {
  v += dpp<0x128>(v); v += dpp<0x124>(v); v += dpp<0x122>(v); v += dpp<0x121>(v);
  return swap32_add(swap16_add(v));
}
__device__ __forceinline__ float wave_max(float v) {
  v = fmaxf(v, dpp<0x128>(v)); v = fmaxf(v, dpp<0x124>(v)); v = fmaxf(v, dpp<0x122>(v)); v = fmaxf(v, dpp<0x121>(v));
  return swap32_max(swap16_max(v));
}
__device__ __forceinline__ f32x4 mfma(u32x4 a, u32x4 b, f32x4 c) {
  return __builtin_amdgcn_mfma_f32_16x16x32_bf16(__builtin_bit_cast(s16x8, a), __builtin_bit_cast(s16x8, b), c, 0, 0, 0);
}
__device__ __forceinline__ void lds_barrier() { asm volatile("s_waitcnt lgkmcnt(0)\n\ts_barrier" ::: "memory"); }
__device__ __forceinline__ void cfence() { asm volatile("" ::: "memory"); __builtin_amdgcn_sched_barrier(0); }

#define LP(T, off) ((LAS T*)(lds + (off)))
#define LDW(p) (*(const GAS u32x4*)(p))

__device__ __forceinline__ void flag_set(u32 addr) {
  asm volatile("ds_write_b32 %0, %1" ::"v"(addr), "v"(1u) : "memory");
}
__device__ __forceinline__ void flag_wait4(u32 addr) {
  for (int it = 0; it < SPIN_MAX; ++it) {
    u32x4 v;
    asm volatile("ds_read_b128 %0, %1\n\ts_waitcnt lgkmcnt(0)" : "=v"(v) : "v"(addr) : "memory");
    if ((v[0] & v[1] & v[2] & v[3]) != 0) break;
    __builtin_amdgcn_s_sleep(1);
  }
}
__device__ __forceinline__ void flag_wait1(u32 addr) {
  for (int it = 0; it < SPIN_MAX; ++it) {
    u32 v;
    asm volatile("ds_read_b32 %0, %1\n\ts_waitcnt lgkmcnt(0)" : "=v"(v) : "v"(addr) : "memory");
    if (v != 0) break;
    __builtin_amdgcn_s_sleep(1);
  }
}
__device__ __forceinline__ u32 pk_tag(float a, float b) {
  return __builtin_amdgcn_perm(pk_bf16(b, b), __float_as_uint(a), 0x07060302u) | 1u;
}
__device__ __forceinline__ bool valid4(const u32x4& v) { return (v[0] & v[1] & v[2] & v[3] & 1u) != 0; }
__device__ __forceinline__ u32x4 maskfrag(u32 d, const u32x4& sel) {
  u32x4 f;
  f[0] = __builtin_amdgcn_perm(d, d, sel[0]); f[1] = __builtin_amdgcn_perm(d, d, sel[1]);
  f[2] = __builtin_amdgcn_perm(d, d, sel[2]); f[3] = __builtin_amdgcn_perm(d, d, sel[3]);
  return f;
}


__global__ __launch_bounds__(512, 2) void mla_ffn_kernel(
    const u16* __restrict__ x, const u16* __restrict__ ckv, const u16* __restrict__ wq, const u16* __restrict__ wuk,
    const u16* __restrict__ wuv, const u16* __restrict__ wo, const u16* __restrict__ w1, const u16* __restrict__ w3,
    const u16* __restrict__ w2, u16* __restrict__ out, unsigned char* __restrict__ ws) {
  __shared__ __attribute__((aligned(16))) unsigned char lds_raw[L_TOTAL];
  LAS unsigned char* lds = (LAS unsigned char*)lds_raw;
  const u32 ldsb = (u32)(uintptr_t)lds;
  const int tid = threadIdx.x, lane = tid & 63;
  const int w = __builtin_amdgcn_readfirstlane(tid >> 6);
  const int bid = blockIdx.x, X = bid & 7, I = bid >> 3;
  const int h = 4 * X + (I >> 3), j = I & 7;
  const int pair = 16 * X + (I >> 1), b = pair >> 6, s = pair & 63, G = I & 1;
  const bool is_final = (I < 16) && ((I & 1) != (X >> 2));
  const int fin = X * 8 + (I >> 1);
  const int fu = 22 * X + (I - 10);
  const bool has_f1 = (I >= 10);
  const bool has_ffn = has_f1 && (fu < 172);
  const int g = lane >> 4, r = lane & 15, jj = lane & 7, bsel = (lane >> 3) & 1;
  if (tid < 44) *LP(u32, L_FLAG + 4 * tid) = 0u;
  asm volatile("" ::"s"(wuv), "s"(wo), "s"(w1), "s"(w3), "s"(w2), "s"(out), "s"(x), "s"(ckv), "s"(wq), "s"(wuk), "s"(ws));
  lds_barrier();
  const __amdgpu_buffer_rsrc_t w2r = __builtin_amdgcn_make_buffer_rsrc((void*)w2, 0, 512 * FF * 2, 0x00020000);
  const __amdgpu_buffer_rsrc_t wsr = __builtin_amdgcn_make_buffer_rsrc((void*)ws, 0, WS_BYTES, 0x00020000);
  const __amdgpu_buffer_rsrc_t wqr = __builtin_amdgcn_make_buffer_rsrc((void*)wq, 0, 512 * 4096 * 2, 0x00020000);
  const __amdgpu_buffer_rsrc_t wor = __builtin_amdgcn_make_buffer_rsrc((void*)wo, 0, 512 * 4096 * 2, 0x00020000);
  const u32 epv = __builtin_amdgcn_raw_buffer_load_b32(wsr, 0, 0, 0);
  u32x4 sel_lo, sel_hi;
#pragma unroll
  for (int k = 0; k < 4; ++k) {
    const bool on = (k == (jj >> 1));
    sel_lo[k] = on ? ((jj & 1) ? 0x01000c0cu : 0x0c0c0100u) : 0x0c0c0c0cu;
    sel_hi[k] = on ? ((jj & 1) ? 0x03020c0cu : 0x0c0c0302u) : 0x0c0c0c0cu;
  }
  const int rot = j & 3;
  u32x4 wreg[32], w2v[2];
  u32 tag;

  if (w < 4) {
    __builtin_amdgcn_s_setprio(2);
    u32x4 wqv[32], wukv[4];
    {
      const u32 vo = ((128 * w + 32 * g) * 4096 + h * 128 + r * 8) * 2;
#pragma unroll
      for (int t = 0; t < 32; ++t) wqv[t] = __builtin_amdgcn_raw_buffer_load_b128(wqr, vo, ((t + 8 * rot) & 31) * 8192, 0);
      const u16* pk = wuk + (size_t)(64 * j + 16 * w + r) * 4096 + h * 128 + g * 8;
#pragma unroll
      for (int k = 0; k < 4; ++k) wukv[k] = LDW(pk + k * 32);
    }
    cfence();
    flag_wait1(ldsb + F_XN);
    u32x4 xf[4];
#pragma unroll
    for (int u = 0; u < 4; ++u) xf[u] = *LP(u32x4, L_XN + bsel * 1024 + (128 * w + 32 * g) * 2 + 16 * ((u + rot) & 3));
    f32x4 q0 = {0.f, 0.f, 0.f, 0.f}, q1 = {0.f, 0.f, 0.f, 0.f};
#pragma unroll
    for (int t = 0; t < 32; t += 2) {
      const u32 d = xf[t >> 3][(t >> 1) & 3];
      q0 = mfma(wqv[t], maskfrag(d, sel_lo), q0);
      q1 = mfma(wqv[t + 1], maskfrag(d, sel_hi), q1);
    }
    q0 += q1;
    *LP(f32x4, L_P + w * 1024 + 16 * lane) = q0;
    flag_set(ldsb + F_Q + 4 * w);
    flag_wait4(ldsb + F_Q);
    {
      f32x4 q = *LP(f32x4, L_P + 16 * lane);
      q += *LP(f32x4, L_P + 1024 + 16 * lane);
      q += *LP(f32x4, L_P + 2048 + 16 * lane);
      q += *LP(f32x4, L_P + 3072 + 16 * lane);
      const u32 p01 = pk_bf16(q[0], q[1]), p23 = pk_bf16(q[2], q[3]);
      LAS u16* qb = LP(u16, L_QB + w * 512 + bsel * 256 + (32 * g + jj) * 2);
      qb[0] = (u16)p01; qb[8] = (u16)(p01 >> 16); qb[16] = (u16)p23; qb[24] = (u16)(p23 >> 16);
    }
    f32x4 qa = {0.f, 0.f, 0.f, 0.f};
#pragma unroll
    for (int k = 0; k < 4; ++k) {
      const u32x4 bq = *LP(u32x4, L_QB + w * 512 + (r & 1) * 256 + (32 * k + 8 * g) * 2);
      qa = mfma(wukv[k], bq, qa);
    }
    const float qs = 0.08838834764831845f * 1.4426950408889634f;
    const u32x2 gr = {pk_tag(qa[0] * qs, qa[1] * qs), pk_tag(qa[2] * qs, qa[3] * qs)};
    if (r < 2) __builtin_amdgcn_raw_buffer_store_b64(gr, wsr, WS_QABS + (r * 32 + h) * 1024 + (64 * j + 16 * w + 4 * g) * 2, 0, 16);
    {
      const u32 ro = WS_READY + (4 * G + w) * 128 + (lane & 7) * 16;
      u32x4 rdy = __builtin_amdgcn_raw_buffer_load_b128(wsr, ro, 0, 16);
      tag = __builtin_amdgcn_readfirstlane(epv) + 1u;
      for (int it = 0; it < SPIN_MAX; ++it) {
        if (__all((rdy[0] == tag) & (rdy[1] == tag) & (rdy[2] == tag) & (rdy[3] == tag))) break;
        __builtin_amdgcn_s_sleep(1);
        rdy = __builtin_amdgcn_raw_buffer_load_b128(wsr, ro, 0, 16);
      }
    }
    flag_wait4(ldsb + F_C3);
    u32x4 gq[4];
    const u32 gqo = WS_QABS + (b * 32 + 16 * G + 4 * w) * 1024 + lane * 16;
#pragma unroll
    for (int u = 0; u < 4; ++u) gq[u] = __builtin_amdgcn_raw_buffer_load_b128(wsr, gqo + u * 1024, 0, 16);
    cfence();
    {
      const u16* pv = wuv + (size_t)(64 * j + 16 * w + g) * 4096 + h * 128 + r * 8;
#pragma unroll
      for (int t = 0; t < 4; ++t) wreg[t] = LDW(pv + 4 * t * 4096);
    }
    cfence();
    for (int it = 0; it < SPIN_MAX; ++it) {
      bool k[4];
#pragma unroll
      for (int u = 0; u < 4; ++u) k[u] = valid4(gq[u]);
      if (__all(k[0] & k[1] & k[2] & k[3])) break;
      __builtin_amdgcn_s_sleep(1);
      u32x4 nw[4];
#pragma unroll
      for (int u = 0; u < 4; ++u) nw[u] = __builtin_amdgcn_raw_buffer_load_b128(wsr, k[u] ? OOB : gqo + u * 1024, 0, 16);
      cfence();
#pragma unroll
      for (int u = 0; u < 4; ++u) gq[u] = k[u] ? gq[u] : nw[u];
    }
    const u32 r2o = WS_READY2 + w * 256 + (lane & 15) * 16;
    u32x4 rdy2 = __builtin_amdgcn_raw_buffer_load_b128(wsr, r2o, 0, 16);
    cfence();
#pragma unroll
    for (int u = 0; u < 4; ++u) {
      const int hl = 4 * w + u;
      *LP(u32x4, L_QA + hl * 1024 + 16 * (lane ^ (2 * (hl & 7)))) = gq[u];
    }
    flag_set(ldsb + F_G + 4 * w);
    u32 qk_a[4], qk_b[4];
#pragma unroll
    for (int k = 0; k < 4; ++k) {
      const u32 o = 16 * ((4 * k + g) ^ (2 * (r & 7)));
      qk_a[k] = L_C + (32 * w + r) * 1024 + o;
      qk_b[k] = L_QA + r * 1024 + o;
    }
    flag_wait4(ldsb + F_G);
    u32x4 bv[16];
#pragma unroll
    for (int kk = 0; kk < 16; ++kk) bv[kk] = *LP(u32x4, qk_b[kk & 3] + (kk >> 2) * 256);
    flag_wait4(ldsb + F_C4);
    f32x4 sc[2];
    {
      f32x4 s0 = {0.f, 0.f, 0.f, 0.f}, s1 = {0.f, 0.f, 0.f, 0.f}, s2 = {0.f, 0.f, 0.f, 0.f}, s3 = {0.f, 0.f, 0.f, 0.f};
      u32x4 a0[8], a1[8];
#pragma unroll
      for (int kk = 0; kk < 8; ++kk) {
        a0[kk] = *LP(u32x4, qk_a[kk & 3] + (kk >> 2) * 256);
        a1[kk] = *LP(u32x4, qk_a[kk & 3] + (kk >> 2) * 256 + 16384);
      }
#pragma unroll
      for (int hb = 0; hb < 2; ++hb) {
        __builtin_amdgcn_sched_barrier(0);
#pragma unroll
        for (int kk = 0; kk < 8; kk += 2) {
          s0 = mfma(a0[kk], bv[8 * hb + kk], s0);
          s2 = mfma(a1[kk], bv[8 * hb + kk], s2);
          s1 = mfma(a0[kk + 1], bv[8 * hb + kk + 1], s1);
          s3 = mfma(a1[kk + 1], bv[8 * hb + kk + 1], s3);
          if (hb == 0) {
#pragma unroll
            for (int e = 0; e < 2; ++e) {
              a0[kk + e] = *LP(u32x4, qk_a[(kk + e) & 3] + (2 + ((kk + e) >> 2)) * 256);
              a1[kk + e] = *LP(u32x4, qk_a[(kk + e) & 3] + (2 + ((kk + e) >> 2)) * 256 + 16384);
            }
          }
        }
      }
      sc[0] = s0 + s1; sc[1] = s2 + s3;
      float m = fmaxf(fmaxf(fmaxf(sc[0][0], sc[0][1]), fmaxf(sc[0][2], sc[0][3])), fmaxf(fmaxf(sc[1][0], sc[1][1]), fmaxf(sc[1][2], sc[1][3])));
      m = swap32_max(swap16_max(m));
      if (g == 0) *LP(float, L_MAX + (r * 4 + w) * 4) = m;
    }
    flag_set(ldsb + F_M + 4 * w);
    u32 pv_a[8];
#pragma unroll
    for (int k = 0; k < 8; ++k)
      pv_a[k] = L_C + (4 * g + (r >> 2)) * 1024 + 16 * ((16 * w + 2 * k + ((r & 3) >> 1)) ^ (2 * (4 * (g & 1) + (r >> 2)))) + 8 * (r & 1);
    flag_wait4(ldsb + F_M);
    float pm;
    {
      const f32x4 m0 = *LP(f32x4, L_MAX + r * 16);
      pm = fmaxf(fmaxf(m0[0], m0[1]), fmaxf(m0[2], m0[3]));
      u32x4 p;
#pragma unroll
      for (int i = 0; i < 2; ++i) {
        p[2 * i] = pk_bf16(__builtin_amdgcn_exp2f(sc[i][0] - pm), __builtin_amdgcn_exp2f(sc[i][1] - pm));
        p[2 * i + 1] = pk_bf16(__builtin_amdgcn_exp2f(sc[i][2] - pm), __builtin_amdgcn_exp2f(sc[i][3] - pm));
      }
      *LP(u32x4, L_P + w * 1024 + lane * 16) = p;
    }
    for (int it = 0; it < SPIN_MAX; ++it) {
      if (__all((rdy2[0] == tag) & (rdy2[1] == tag) & (rdy2[2] == tag) & (rdy2[3] == tag))) break;
      __builtin_amdgcn_s_sleep(1);
      rdy2 = __builtin_amdgcn_raw_buffer_load_b128(wsr, r2o, 0, 16);
    }
    flag_set(ldsb + F_P + 4 * w);
    {
      s16x4 lo[16], hi[16];
#pragma unroll
      for (int k2 = 0; k2 < 2; ++k2)
#pragma unroll
        for (int ct = 0; ct < 8; ++ct) {
          lo[k2 * 8 + ct] = __builtin_amdgcn_ds_read_tr16_b64_v4i16(LP(s16x4, pv_a[ct] + k2 * 32768));
          hi[k2 * 8 + ct] = __builtin_amdgcn_ds_read_tr16_b64_v4i16(LP(s16x4, pv_a[ct] + k2 * 32768 + 16384));
        }
      flag_wait4(ldsb + F_P);
      u32x4 pf[4];
#pragma unroll
      for (int ks = 0; ks < 4; ++ks) pf[ks] = *LP(u32x4, L_P + ks * 1024 + lane * 16);
      f32x4 acc[8], accl = {0.f, 0.f, 0.f, 0.f};
      const u32x4 ones = {0x3f803f80u, 0x3f803f80u, 0x3f803f80u, 0x3f803f80u};
#pragma unroll
      for (int ks = 0; ks < 4; ++ks) accl = mfma(ones, pf[ks], accl);
      const float pl = accl[0];
      const float inv = __builtin_amdgcn_rcpf(pl);
      if (w == 3 && g == 0) {
        const float lse = pm + __builtin_amdgcn_logf(pl);
        __builtin_amdgcn_raw_buffer_store_b32(__float_as_uint(lse) | 1u, wsr, WS_LSE + (16 * G + r) * 512 + (s * 2 + b) * 4, 0, 16);
      }
#pragma unroll
      for (int ct = 0; ct < 8; ++ct) acc[ct] = f32x4{0.f, 0.f, 0.f, 0.f};
#pragma unroll
      for (int hb = 0; hb < 2; ++hb) {
        __builtin_amdgcn_sched_barrier(0);
#pragma unroll
        for (int k2 = 0; k2 < 2; ++k2)
#pragma unroll
          for (int ct = 0; ct < 8; ++ct) {
            const u32x2 l2 = __builtin_bit_cast(u32x2, lo[k2 * 8 + ct]), h2 = __builtin_bit_cast(u32x2, hi[k2 * 8 + ct]);
            acc[ct] = mfma(u32x4{l2[0], l2[1], h2[0], h2[1]}, pf[2 * hb + k2], acc[ct]);
            if (hb == 0) {
              lo[k2 * 8 + ct] = __builtin_amdgcn_ds_read_tr16_b64_v4i16(LP(s16x4, pv_a[ct] + (2 + k2) * 32768));
              hi[k2 * 8 + ct] = __builtin_amdgcn_ds_read_tr16_b64_v4i16(LP(s16x4, pv_a[ct] + (2 + k2) * 32768 + 16384));
            }
          }
      }
      const u32 stg = L_QA + w * 4096;
#pragma unroll
      for (int q4 = 0; q4 < 4; ++q4) {
        u32x4 o;
        o[0] = pk_tag(acc[2 * q4][0] * inv, acc[2 * q4][1] * inv);
        o[1] = pk_tag(acc[2 * q4][2] * inv, acc[2 * q4][3] * inv);
        o[2] = pk_tag(acc[2 * q4 + 1][0] * inv, acc[2 * q4 + 1][1] * inv);
        o[3] = pk_tag(acc[2 * q4 + 1][2] * inv, acc[2 * q4 + 1][3] * inv);
        *LP(u32x4, stg + (q4 >> 1) * 2048 + r * 128 + 16 * (((q4 & 1) * 4 + g) ^ (r & 7))) = o;
      }
#pragma unroll
      for (int q4 = 0; q4 < 4; ++q4) {
        const int hd = 8 * (q4 & 1) + (lane >> 3);
        const u32x4 o = *LP(u32x4, stg + (q4 >> 1) * 2048 + hd * 128 + 16 * ((lane & 7) ^ (hd & 7)));
        __builtin_amdgcn_raw_buffer_store_b128(o, wsr, WS_INA + ((16 * G + hd) * 8 + 2 * w + (q4 >> 1)) * INA_STRIDE + (b * 64 + s) * 128 + (lane & 7) * 16, 0, 16);
      }
    }
    flag_set(ldsb + F_PV + 4 * w);
  } else {
    const int u = w - 4;
    const u32x4 z = {0u, 0u, 0u, 0u};
    u32x4 x0, x1;
    if (w == 7) {
      __builtin_amdgcn_s_setprio(3);
      if (lane < 16) __builtin_amdgcn_raw_buffer_store_b128(z, wsr, WS_QABS + ((lane >> 3) * 32 + h) * 1024 + j * 128 + (lane & 7) * 16, 0, 16);
      cfence();
      x0 = LDW(x + 8 * lane); x1 = LDW(x + 512 + 8 * lane);
    }
    if (w == 7) {
      float a[8], c[8];
#pragma unroll
      for (int k = 0; k < 4; ++k) { a[2 * k] = bf_lo(x0[k]); a[2 * k + 1] = bf_hi(x0[k]); c[2 * k] = bf_lo(x1[k]); c[2 * k + 1] = bf_hi(x1[k]); }
      float sa = ((a[0] + a[1]) + (a[2] + a[3])) + ((a[4] + a[5]) + (a[6] + a[7]));
      float sc = ((c[0] + c[1]) + (c[2] + c[3])) + ((c[4] + c[5]) + (c[6] + c[7]));
      sa = wave_sum(sa) * (1.f / 512.f); sc = wave_sum(sc) * (1.f / 512.f);
      float va = 0.f, vc = 0.f, vb = 0.f, vd = 0.f;
#pragma unroll
      for (int k = 0; k < 8; k += 2) {
        const float da = a[k] - sa, dc = c[k] - sc, db = a[k + 1] - sa, dd = c[k + 1] - sc;
        va += da * da; vc += dc * dc; vb += db * db; vd += dd * dd;
      }
      va = wave_sum(va + vb) * (1.f / 512.f); vc = wave_sum(vc + vd) * (1.f / 512.f);
      const float ma = rbf(sa), mc = rbf(sc);
      const float ra = __builtin_amdgcn_rcpf(rbf(__builtin_amdgcn_sqrtf(rbf(rbf(va) + 1e-5f))));
      const float rc = __builtin_amdgcn_rcpf(rbf(__builtin_amdgcn_sqrtf(rbf(rbf(vc) + 1e-5f))));
      u32x4 n0, n1;
#pragma unroll
      for (int k = 0; k < 4; ++k) {
        const u32 da = pk_bf16(a[2 * k] - ma, a[2 * k + 1] - ma), dc = pk_bf16(c[2 * k] - mc, c[2 * k + 1] - mc);
        n0[k] = pk_bf16(bf_lo(da) * ra, bf_hi(da) * ra);
        n1[k] = pk_bf16(bf_lo(dc) * rc, bf_hi(dc) * rc);
      }
      *LP(u32x4, L_XN + 16 * lane) = n0;
      *LP(u32x4, L_XN + 1024 + 16 * lane) = n1;
      const u32 t7 = __builtin_amdgcn_readfirstlane(epv) + 1u;
      asm volatile("s_waitcnt vmcnt(0)" ::: "memory");
      flag_set(ldsb + F_XN);
      if (lane == 0) __builtin_amdgcn_raw_buffer_store_b32(t7, wsr, WS_READY + (X * 32 + I) * 4, 0, 16);
      __builtin_amdgcn_s_setprio(1);
    }
    {
      const u32 ia = WS_INA + (h * 8 + j) * INA_STRIDE + u * 4096 + lane * 16;
#pragma unroll
      for (int k = 0; k < 4; ++k) __builtin_amdgcn_raw_buffer_store_b128(z, wsr, ia, k * 1024, 16);
      if (w == 5 && j == 0 && lane < 32) __builtin_amdgcn_raw_buffer_store_b128(z, wsr, WS_LSE + h * 512 + lane * 16, 0, 16);
      if (is_final) {
        const u32 ib = WS_INB + fin * 16384 + u * 4096 + lane * 16;
#pragma unroll
        for (int k = 0; k < 4; ++k) __builtin_amdgcn_raw_buffer_store_b128(z, wsr, ib, k * 1024, 16);
      }
    }
    cfence();
    const u16* cbase = ckv + ((size_t)b * SK + s * 128 + 32 * u) * 512;
#pragma unroll
    for (int i = 0; i < 32; ++i) {
      const u32 row = (u32)i ^ ((u32)G << 4);
      const u32 voff = row * 1024 + 16 * (lane ^ (2 * (i & 7)));
      const u32 dst = ldsb + L_C + (32 * u) * 1024 + row * 1024;
      asm volatile("s_mov_b32 m0, %2\n\ts_nop 0\n\tglobal_load_lds_dwordx4 %0, %1" ::"v"(voff), "s"(cbase), "s"(dst) : "memory");
      if (i == 7) {
        asm volatile("s_waitcnt vmcnt(8)" ::: "memory");
        flag_set(ldsb + F_Z + 4 * u);
      }
      if (i == 15 && w == 4) {
        u32 e2;
        asm volatile("s_load_dword %0, %1, 0x0\n\ts_waitcnt lgkmcnt(0)" : "=s"(e2) : "s"(ws) : "memory");
        flag_wait4(ldsb + F_Z);
        if (lane == 0) __builtin_amdgcn_raw_buffer_store_b32(e2 + 1u, wsr, WS_READY2 + bid * 4, 0, 16);
      }
    }
    __builtin_amdgcn_s_setprio(0);
    asm volatile("s_waitcnt vmcnt(8)" ::: "memory");
    flag_set(ldsb + F_C3 + 4 * u);
    u32x4 wf1[4];
    {
      const u32 vo = ((h * 128 + 32 * g) * 512 + 128 * u + 8 * r) * 2;
#pragma unroll
      for (int t = 0; t < 4; ++t) wreg[t] = __builtin_amdgcn_raw_buffer_load_b128(wor, vo, ((t + 8 * rot) & 31) * 1024, 0);
      cfence();
      asm volatile("s_waitcnt vmcnt(4)" ::: "memory");
      flag_set(ldsb + F_C4 + 4 * u);
      cfence();
      tag = __builtin_amdgcn_readfirstlane(epv) + 1u;
      asm volatile("" : "+s"(tag)::"memory");
      cfence();
#pragma unroll
      for (int t = 0; t < 2; ++t)
        w2v[t] = __builtin_amdgcn_raw_buffer_load_b128(w2r, has_ffn ? (u32)((8 * fu + 4 * t + g) * 512 + 128 * u + 8 * r) * 2u : OOB, 0, 0);
#pragma unroll
      for (int t = 4; t < 32; ++t) wreg[t] = __builtin_amdgcn_raw_buffer_load_b128(wor, vo, ((t + 8 * rot) & 31) * 1024, 0);
      cfence();
      if (has_f1) {
        int ch = r & 7;
        if ((fu >> 3) == 21) ch &= 3;
        const u16* fp = ((r >> 3) ? w3 : w1) + (size_t)(64 * (fu & 7) + 16 * u + g) * FF + 64 * (fu >> 3) + 8 * ch;
#pragma unroll
        for (int t = 0; t < 4; ++t) wf1[t] = LDW(fp + 4 * t * FF);
      }
      cfence();
    }
    cfence();
    if (has_f1) {
      flag_wait1(ldsb + F_XN);
      f32x4 fa = {0.f, 0.f, 0.f, 0.f};
#pragma unroll
      for (int t = 0; t < 4; ++t) {
        const u32 d = *LP(u16, L_XN + bsel * 1024 + (64 * (fu & 7) + 16 * u + 4 * t + g) * 2);
        fa = mfma(wf1[t], maskfrag(d, sel_lo), fa);
      }
      *LP(f32x4, L_F1 + u * 1024 + lane * 16) = fa;
      flag_set(ldsb + F_F + 4 * u);
      flag_wait4(ldsb + F_F);
      const LAS float* fl = LP(float, L_F1 + lane * 16 + u * 4);
      const float v = (fl[0] + fl[256]) + (fl[512] + fl[768]);
      __builtin_amdgcn_raw_buffer_store_b64(u32x2{__float_as_uint(v), tag}, wsr,
          WS_INF + ((fu & ~7) + 4 * (g & 1) + u) * 2048 + (fu & 7) * 256 + ((g >> 1) * 16 + r) * 8, 0, 16);
    }
    flag_wait4(ldsb + F_PV);
  }

  {
    const u32 po = WS_INA + (h * 8 + j) * INA_STRIDE + (bsel * 64 + 8 * w + g) * 128 + jj * 16;
    f32x4 o0 = {0.f, 0.f, 0.f, 0.f}, o1 = {0.f, 0.f, 0.f, 0.f};
    u32x4 pc0, pc1; u32x2 la; u32 l0, l1;
    const u32 lo_ = WS_LSE + h * 512 + lane * 8;
    if (w == 0) {
      la = __builtin_amdgcn_raw_buffer_load_b64(wsr, lo_, 0, 16);
      for (int it = 0; it < SPIN_MAX; ++it) {
        const bool k2 = (la[0] & la[1] & 1u) != 0;
        if (__all(k2)) break;
        __builtin_amdgcn_s_sleep(2);
        const u32x2 n2 = __builtin_amdgcn_raw_buffer_load_b64(wsr, k2 ? OOB : lo_, 0, 16);
        cfence();
        la = k2 ? la : n2;
      }
      *LP(u32x2, L_QB + lane * 8) = la;
      flag_set(ldsb + F_L);
    } else {
      flag_wait1(ldsb + F_L);
      la = *LP(u32x2, L_QB + lane * 8);
    }
    u32x4 n0, n1;
    bool k0, k1;
    pc0 = __builtin_amdgcn_raw_buffer_load_b128(wsr, po, 0, 16);
    pc1 = __builtin_amdgcn_raw_buffer_load_b128(wsr, po + 512, 0, 16);
    const u32 fo = (has_ffn && w >= 4) ? WS_INF + fu * 2048 + lane * 16 : OOB;
    u32x4 f0 = __builtin_amdgcn_raw_buffer_load_b128(wsr, fo, 0, 16);
    u32x4 f1 = __builtin_amdgcn_raw_buffer_load_b128(wsr, fo, 1024, 16);
    cfence();
    const float e0 = __uint_as_float(la[0]), e1 = __uint_as_float(la[1]);
    const float m0 = wave_max(e0), m1 = wave_max(e1);
    const float a0 = rbf(__builtin_amdgcn_exp2f(e0 - m0)), a1 = rbf(__builtin_amdgcn_exp2f(e1 - m1));
    const u32 ap = pk_bf16(a0, a1);
    l0 = (u32)__builtin_amdgcn_ds_bpermute((8 * w + g) * 4, (int)ap);
    l1 = (u32)__builtin_amdgcn_ds_bpermute((8 * w + 4 + g) * 4, (int)ap);
    const float L0 = wave_sum(a0), L1 = wave_sum(a1);
    const u32x4 sl = bsel ? sel_hi : sel_lo;
    const u32x4 fr0 = maskfrag(l0, sl), fr1 = maskfrag(l1, sl);
    flag_wait4(ldsb + F_PV);
    cfence();
    for (int it = 0; it < SPIN_MAX; ++it) {
      k0 = valid4(pc0); k1 = valid4(pc1);
      if (__all(k0 && k1)) break;
      n0 = __builtin_amdgcn_raw_buffer_load_b128(wsr, k0 ? OOB : po, 0, 16);
      n1 = __builtin_amdgcn_raw_buffer_load_b128(wsr, k1 ? OOB : po + 512, 0, 16);
      cfence();
      pc0 = k0 ? pc0 : n0; pc1 = k1 ? pc1 : n1;
    }
    f32x4 d1 = {0.f, 0.f, 0.f, 0.f};
    d1 = mfma(pc0, fr0, d1);
    d1 = mfma(pc1, fr1, d1);
    if (bsel == (g >> 1)) {
      LAS float* p = LP(float, L_R1 + ((bsel * 64 + 32 * (g & 1) + 16 * (jj >> 2) + (jj & 3)) * 8 + w) * 4);
      p[0] = d1[0]; p[32] = d1[1]; p[64] = d1[2]; p[96] = d1[3];
    }
    lds_barrier();
    if (w < 4) {
      const float invL = __builtin_amdgcn_rcpf(bsel ? L1 : L0);
      f32x4 d2 = {0.f, 0.f, 0.f, 0.f}, d3 = {0.f, 0.f, 0.f, 0.f};
#pragma unroll
      for (int t = 0; t < 4; ++t) {
        const f32x4 u0 = *LP(f32x4, L_R1 + (bsel * 64 + 16 * w + 4 * t + g) * 32);
        const f32x4 u1 = *LP(f32x4, L_R1 + (bsel * 64 + 16 * w + 4 * t + g) * 32 + 16);
        const float v = (((u0[0] + u0[1]) + (u0[2] + u0[3])) + ((u1[0] + u1[1]) + (u1[2] + u1[3]))) * invL;
        if (t & 1) d3 = mfma(wreg[t], maskfrag(pk_bf16(v, v), sel_lo), d3);
        else d2 = mfma(wreg[t], maskfrag(pk_bf16(v, v), sel_lo), d2);
      }
      d2 += d3;
      LAS float* p = LP(float, L_R2 + (w * 320 + bsel * 160 + 40 * g + jj) * 4);
      p[0] = d2[0]; p[8] = d2[1]; p[16] = d2[2]; p[24] = d2[3];
    } else if (has_ffn) {
      for (int it = 0; it < SPIN_MAX; ++it) {
        if (__all((f0[1] == tag) & (f0[3] == tag) & (f1[1] == tag) & (f1[3] == tag))) break;
        __builtin_amdgcn_s_sleep(1);
        f0 = __builtin_amdgcn_raw_buffer_load_b128(wsr, fo, 0, 16);
        f1 = __builtin_amdgcn_raw_buffer_load_b128(wsr, fo, 1024, 16);
      }
      const float t0 = swap32_add(swap16_add(__uint_as_float(f0[0]) + __uint_as_float(f1[0])));
      const float t1 = swap32_add(swap16_add(__uint_as_float(f0[2]) + __uint_as_float(f1[2])));
      const float g0 = rbf(t0), g1 = rbf(t1);
      const float u0 = dpp<0x128>(g0), u1 = dpp<0x128>(g1);
      const float s0 = rbf(g0 * __builtin_amdgcn_rcpf(1.f + __expf(-g0))), s1 = rbf(g1 * __builtin_amdgcn_rcpf(1.f + __expf(-g1)));
      const u32 hv = pk_bf16(s0 * u0, s1 * u1);
      const u32 h0 = (u32)__builtin_amdgcn_ds_bpermute((bsel * 4 + (g >> 1)) * 4, (int)hv);
      const u32 h1 = (u32)__builtin_amdgcn_ds_bpermute((bsel * 4 + 2 + (g >> 1)) * 4, (int)hv);
      const u32x4 sf = (g & 1) ? sel_hi : sel_lo;
      o0 = mfma(w2v[0], maskfrag(h0, sf), o0);
      o1 = mfma(w2v[1], maskfrag(h1, sf), o1);
    }
    lds_barrier();
    if (w >= 4) {
      const int u = w - 4;
      const u32 ro = L_R2 + ((lane >> 5) * 160 + 40 * ((lane & 31) >> 3) + 4 * (lane & 7)) * 4;
      f32x4 cs[4];
#pragma unroll
      for (int k = 0; k < 4; ++k) cs[k] = *LP(f32x4, ro + k * 1280);
      const f32x4 c = (cs[0] + cs[1]) + (cs[2] + cs[3]);
      *LP(u32x2, L_QB + u * 512 + lane * 8) = u32x2{pk_bf16(c[0], c[1]), pk_bf16(c[2], c[3])};
      u32x4 cf[4];
#pragma unroll
      for (int k = 0; k < 4; ++k) cf[k] = *LP(u32x4, L_QB + u * 512 + bsel * 256 + g * 64 + 16 * ((k + rot) & 3));
      f32x4 o2 = {0.f, 0.f, 0.f, 0.f}, o3 = {0.f, 0.f, 0.f, 0.f};
#pragma unroll
      for (int t = 0; t < 32; t += 4) {
        const u32 d = cf[t >> 3][(t >> 1) & 3], e = cf[t >> 3][((t >> 1) & 3) + 1];
        o0 = mfma(wreg[t], maskfrag(d, sel_lo), o0);
        o1 = mfma(wreg[t + 1], maskfrag(d, sel_hi), o1);
        o2 = mfma(wreg[t + 2], maskfrag(e, sel_lo), o2);
        o3 = mfma(wreg[t + 3], maskfrag(e, sel_hi), o3);
      }
      o0 = (o0 + o1) + (o2 + o3);
      u32x4 o;
#pragma unroll
      for (int k = 0; k < 4; ++k) o[k] = __float_as_uint(o0[k]) | 1u;
      const u32 f = ((bsel * 4 + u) * 4 + g) * 2 + (jj >> 2);
      __builtin_amdgcn_raw_buffer_store_b128(o, wsr, WS_INB + f * 16384 + bid * 64 + (jj & 3) * 16, 0, 16);
    }
  }

  if (is_final) {
    const int fb = fin >> 5, fq = (fin >> 3) & 3, fg = (fin >> 1) & 3, fa = fin & 1;
    const int n = 128 * fq + 32 * fg + 8 * (tid & 3) + 4 * fa + ((tid >> 2) & 3);
    const float xv = bf_lo((u32)x[fb * 512 + n]);
    const u32 ib = WS_INB + fin * 16384 + (16 * w + (lane >> 2)) * 64 + (lane & 3) * 16;
    u32x4 s0 = __builtin_amdgcn_raw_buffer_load_b128(wsr, ib, 0, 16);
    u32x4 s1 = __builtin_amdgcn_raw_buffer_load_b128(wsr, ib + 8192, 0, 16);
    for (int it = 0; it < SPIN_MAX; ++it) {
      const bool k0 = (s0[0] & s0[1] & s0[2] & s0[3] & 1u) != 0, k1 = (s1[0] & s1[1] & s1[2] & s1[3] & 1u) != 0;
      if (__all(k0 && k1)) break;
      __builtin_amdgcn_s_sleep(1);
      const u32x4 n0 = __builtin_amdgcn_raw_buffer_load_b128(wsr, k0 ? OOB : ib, 0, 16);
      const u32x4 n1 = __builtin_amdgcn_raw_buffer_load_b128(wsr, k1 ? OOB : ib + 8192, 0, 16);
      cfence();
      s0 = k0 ? s0 : n0; s1 = k1 ? s1 : n1;
    }
    f32x4 v;
#pragma unroll
    for (int k = 0; k < 4; ++k) {
      float t = __uint_as_float(s0[k]) + __uint_as_float(s1[k]);
      t += dpp<0x124>(t); t += dpp<0x128>(t);
      v[k] = swap32_add(swap16_add(t));
    }
    if (lane < 4) *LP(f32x4, L_R4 + w * 64 + lane * 16) = v;
    lds_barrier();
    if (tid < 16) {
      float t = 0.f;
#pragma unroll
      for (int k = 0; k < 8; ++k) t += *LP(float, L_R4 + k * 64 + (tid >> 2) * 16 + (tid & 3) * 4);
      out[fb * 512 + n] = (u16)pk_bf16(xv + t, 0.f);
    }
    if (fin == 0 && tid == 0) __builtin_amdgcn_raw_buffer_store_b32(tag, wsr, 0, 0, 16);
  }
}

extern "C" void kernel_launch(void* const* d_in, const int* in_sizes, int n_in, void* d_out, int out_size, void* d_ws,
                              size_t ws_size, hipStream_t stream) {
  mla_ffn_kernel<<<256, 512, 0, stream>>>((const u16*)d_in[0], (const u16*)d_in[1], (const u16*)d_in[2], (const u16*)d_in[3],
                                          (const u16*)d_in[4], (const u16*)d_in[5], (const u16*)d_in[7], (const u16*)d_in[8],
                                          (const u16*)d_in[9], (u16*)d_out, (unsigned char*)d_ws);
}
```
